```python
import torch

def _softmax(x, axis=-1):
    return torch.softmax(x, dim=axis)
def _gelu(x):
    return torch.nn.functional.gelu(x, approximate="tanh")
def _silu(x):
    return torch.nn.functional.silu(x)
def _sigmoid(x):
    return torch.sigmoid(x)
def _layernorm(x, axis=-1, eps=1e-5):
    m = x.mean(dim=axis, keepdim=True)
    v = x.var(dim=axis, keepdim=True, unbiased=False)
    return (x - m) / torch.sqrt(v + eps)
def _scatter_add(tgt, idx, src, axis=0):
    return tgt.clone().index_add_(axis, idx, src)
def _segment_sum(src, seg, n):
    out = torch.zeros((n,) + src.shape[1:], dtype=src.dtype)
    return out.index_add_(0, seg, src)
def _pad(x, pw):
    flat = tuple(v for pair in reversed(pw) for v in pair)
    return torch.nn.functional.pad(x, flat)

kernel_name = 'standalone__paged_block_table_kv_pagedattention__sampling'

def setup_inputs():

    _w = (lambda a: (lambda t: t.to(torch.bfloat16) if t.is_floating_point() else t)(torch.as_tensor(a)))
    B, Hq, Hkv, Sq, Skv, D = 16, 32, 4, 8, 4096, 128
    bs = 16
    nbps = max(1, Skv // bs)
    num_phys = B * nbps + 4
    bt = torch.randperm(num_phys)[: B * nbps].reshape(B, nbps).to(torch.int64)
    cl = (nbps * bs - (torch.arange(1, B + 1, dtype=torch.int64) * 2654435761) % max(1, nbps * bs // 2)).to(torch.int64)
    return {
        "q": _w((lambda *s: torch.randn(*s, dtype=torch.bfloat16) * 0.8)(B, Hq, Sq, D)),
        "k_cache": _w((lambda *s: torch.randn(*s, dtype=torch.bfloat16) * 0.8)(num_phys, bs, Hkv, D)),
        "v_cache": _w((lambda *s: torch.randn(*s, dtype=torch.bfloat16) * 0.05)(num_phys, bs, Hkv, D)),
        "block_table": _w(bt),
        "context_lens": _w(cl),
    }

def reference(q, k_cache, v_cache, block_table, context_lens):
    B, Hq, Sq, D = q.shape
    bs = k_cache.shape[1]
    Hkv = k_cache.shape[2]
    nbps = block_table.shape[1]
    Skv = nbps * bs
    bt = block_table.reshape(-1)
    k = (lambda x, *d: x.permute(*d))(k_cache[bt].reshape(B, Skv, Hkv, D), 0, 2, 1, 3)
    v = (lambda x, *d: x.permute(*d))(v_cache[bt].reshape(B, Skv, Hkv, D), 0, 2, 1, 3)
    g = Hq // Hkv
    qg = q.reshape(B, Hkv, g, Sq, D)
    scores = torch.einsum("bhgqd,bhkd->bhgqk", qg, k) * (D ** -0.5)
    pos = torch.arange(Skv)
    msk = pos[None, :] < context_lens[:, None]
    scores = torch.where(msk[:, None, None, None, :], scores, -1e9)
    p = _softmax(scores, axis=-1)
    out = torch.einsum("bhgqk,bhkd->bhgqd", p, v)
    return out.reshape(B, Hq, Sq, D)
```

```cpp
#include <hip/hip_runtime.h>
#include <hip/hip_bf16.h>
#include <stdint.h>

namespace {

constexpr int kB = 16, kHQ = 32, kHKV = 4, kSQ = 8, kD = 128, kBS = 16, kNBPS = 256;
constexpr int kG = kHQ / kHKV;
constexpr int kM = kG * kSQ;
constexpr int kSKV = kNBPS * kBS;
constexpr int kTile = 32;
constexpr int kNWG = 256;
constexpr int kSlots = kNWG / kHKV;
constexpr int kWaveLds = 40960;
constexpr int kLdsBytes = 4 * kWaveLds;
constexpr int kOPitch = 132;

using bf16x8 = __attribute__((ext_vector_type(8))) __bf16;
using bf16x4 = __attribute__((ext_vector_type(4))) __bf16;
using bf16x2 = __attribute__((ext_vector_type(2))) __bf16;
using f32x16 = __attribute__((ext_vector_type(16))) float;
using f32x4 = __attribute__((ext_vector_type(4))) float;
using f32x2 = __attribute__((ext_vector_type(2))) float;
using u32x4 = __attribute__((ext_vector_type(4))) unsigned;
typedef __attribute__((address_space(3))) bf16x4* lds_bf16x4_ptr;

__device__ __forceinline__ unsigned off_b(unsigned row, unsigned ch) {
  return 256u * row + 16u * (ch ^ (((row & 3u) << 2) | ((row >> 2) & 3u)));
}

__device__ __forceinline__ int eff_len(int c) { return c <= 0 ? kSKV : (c > kSKV ? kSKV : c); }
__device__ __forceinline__ int row_scan(int v) {
  v += __builtin_amdgcn_update_dpp(0, v, 0x111, 0xf, 0xf, true);
  v += __builtin_amdgcn_update_dpp(0, v, 0x112, 0xf, 0xf, true);
  v += __builtin_amdgcn_update_dpp(0, v, 0x114, 0xf, 0xf, true);
  v += __builtin_amdgcn_update_dpp(0, v, 0x118, 0xf, 0xf, true);
  return v;
}
struct Plan {
  int nt, ns, sc, c;
  int row;
};
__device__ __forceinline__ void make_plan(const int* __restrict__ cl, int lane, Plan& p) {
  p.c = cl[lane & 15];
  p.nt = (eff_len(p.c) + kTile - 1) >> 5;
  const int total = __builtin_amdgcn_readlane(row_scan(p.nt), 15);
  int C0 = (total + kSlots - 1) / kSlots;
  for (;;) {
    const unsigned cand = (unsigned)(C0 + (lane >> 4));
    p.ns = (int)(((unsigned)p.nt + cand - 1u) / cand);
    p.sc = row_scan(p.ns);
    const unsigned long long ok = __ballot((lane & 15) == 15 && p.sc <= kSlots);
    if (ok) { p.row = (__ffsll((long long)ok) - 1) >> 4; break; }
    C0 += 4;
  }
}

#define WAIT_VM(n) asm volatile("s_waitcnt vmcnt(" #n ")" ::: "memory")
#define WAIT_LGKM0() asm volatile("s_waitcnt lgkmcnt(0)" ::: "memory")

__global__ void __launch_bounds__(256, 1)
attn_split_kernel(const __hip_bfloat16* __restrict__ q, const __hip_bfloat16* __restrict__ kc,
                  const __hip_bfloat16* __restrict__ vc, const int* __restrict__ bt,
                  const int* __restrict__ cl, float* __restrict__ po, float* __restrict__ pml) {
  extern __shared__ __attribute__((aligned(16))) char lds[];
  const int tid = threadIdx.x, lane = tid & 63;
  const int wave = __builtin_amdgcn_readfirstlane(tid >> 6);
  const int r32 = lane & 31, h = lane >> 5;
  const int kvh = blockIdx.x & (kHKV - 1), slot = blockIdx.x / kHKV;

  Plan p;
  make_plan(cl, lane, p);
  int b, tile0, tile1, clb;
  {
    const unsigned long long hit = __ballot((lane >> 4) == p.row && slot >= p.sc - p.ns && slot < p.sc);
    if (!hit) return;
    const int li = __ffsll((long long)hit) - 1;
    b = li & 15;
    const int nt = __builtin_amdgcn_readlane(p.nt, li), ns = __builtin_amdgcn_readlane(p.ns, li);
    const int j = slot - (__builtin_amdgcn_readlane(p.sc, li) - ns);
    clb = __builtin_amdgcn_readlane(p.c, li);
    const int base = nt / ns, rem = nt - base * ns;
    tile0 = j * base + (j < rem ? j : rem);
    tile1 = tile0 + base + (j < rem ? 1 : 0);
  }
  const int len = eff_len(clb);
  const float c2 = clb > 0 ? 0.08838834764831845f * 1.4426950408889634f : 0.f;

  const int n = (tile1 - tile0 - wave + 3) >> 2;
  int btv = 0;
  {
    const int pg = 2 * (tile0 + wave + 4 * (lane >> 1)) + (lane & 1);
    if (pg < kNBPS) btv = bt[b * kNBPS + pg];
  }
  bf16x8 qf[2][8];
  {
    const __hip_bfloat16* qb = q + (size_t)(b * kHQ + kvh * kG) * kSQ * kD;
#pragma unroll
    for (int rb = 0; rb < 2; ++rb)
#pragma unroll
      for (int s = 0; s < 8; ++s)
        qf[rb][s] = *reinterpret_cast<const bf16x8*>(qb + (size_t)(32 * rb + r32) * kD + 16 * s + 8 * h);
  }

  unsigned voff[4];
#pragma unroll
  for (int i = 0; i < 4; ++i)
    voff[i] = (unsigned)(4 * i + (lane >> 4)) * 1024u + ((unsigned)((lane & 15) ^ (((lane >> 4) << 2) | i)) << 4);

  char* const wl = lds + wave * kWaveLds;
  const char* const kbase = reinterpret_cast<const char*>(kc) + kvh * 256;
  const char* const vbase = reinterpret_cast<const char*>(vc) + kvh * 256;

  auto issue = [&](const char* gb, int it, unsigned dst) {
    const unsigned pa = (unsigned)__builtin_amdgcn_readlane(btv, 2 * it);
    const unsigned pb = (unsigned)__builtin_amdgcn_readlane(btv, 2 * it + 1);
    const char* ga = gb + (size_t)pa * 16384u;
    const char* gbb = gb + (size_t)pb * 16384u;
    asm volatile(
        "s_nop 4\n\t"
        "s_mov_b32 m0, %0\n\ts_nop 0\n\tglobal_load_lds_dwordx4 %3, %1\n\t"
        "s_add_u32 m0, m0, 1024\n\ts_nop 0\n\tglobal_load_lds_dwordx4 %4, %1\n\t"
        "s_add_u32 m0, m0, 1024\n\ts_nop 0\n\tglobal_load_lds_dwordx4 %5, %1\n\t"
        "s_add_u32 m0, m0, 1024\n\ts_nop 0\n\tglobal_load_lds_dwordx4 %6, %1\n\t"
        "s_add_u32 m0, m0, 1024\n\ts_nop 0\n\tglobal_load_lds_dwordx4 %3, %2\n\t"
        "s_add_u32 m0, m0, 1024\n\ts_nop 0\n\tglobal_load_lds_dwordx4 %4, %2\n\t"
        "s_add_u32 m0, m0, 1024\n\ts_nop 0\n\tglobal_load_lds_dwordx4 %5, %2\n\t"
        "s_add_u32 m0, m0, 1024\n\ts_nop 0\n\tglobal_load_lds_dwordx4 %6, %2"
        :
        : "s"(dst), "s"(ga), "s"(gbb), "v"(voff[0]), "v"(voff[1]), "v"(voff[2]), "v"(voff[3])
        : "memory", "scc");
  };
  const unsigned wl_off = (unsigned)(uintptr_t)lds + (unsigned)wave * kWaveLds;

  f32x16 o[4][2];
#pragma unroll
  for (int d = 0; d < 4; ++d)
#pragma unroll
    for (int rb = 0; rb < 2; ++rb)
#pragma unroll
      for (int r = 0; r < 16; ++r) o[d][rb][r] = 0.f;
  float m[2] = {-INFINITY, -INFINITY}, l[2] = {0.f, 0.f};

#pragma unroll
  for (int rb = 0; rb < 2; ++rb)
#pragma unroll
    for (int s = 0; s < 8; ++s) asm volatile("" ::"v"(qf[rb][s]));
  asm volatile("" ::"v"(btv));
  if (n > 0) { issue(kbase, 0, wl_off); issue(vbase, 0, wl_off + 16384); }
  if (n > 1) { issue(kbase, 1, wl_off + 8192); issue(vbase, 1, wl_off + 24576); }

  unsigned kro[8];
#pragma unroll
  for (int s = 0; s < 8; ++s) kro[s] = off_b(r32, 2 * s + h);
  unsigned vro[4][2][2];
  {
    const int blk = (lane >> 4) & 1, qq = (lane & 15) >> 2, pp = lane & 3;
#pragma unroll
    for (int d = 0; d < 4; ++d)
#pragma unroll
      for (int ks = 0; ks < 2; ++ks)
#pragma unroll
        for (int t = 0; t < 2; ++t)
          vro[d][ks][t] = off_b(16 * ks + 8 * t + 4 * h + qq, 4 * d + 2 * blk + (pp >> 1)) + 8 * (pp & 1);
  }

  for (int it = 0; it < n; ++it) {
    char* kslot = wl + (it & 1) * 8192;
    char* vslot = wl + 16384 + (it & 1) * 8192;
    if (it + 1 < n) WAIT_VM(24); else WAIT_VM(8);
    bf16x8 kf[8];
#pragma unroll
    for (int s = 0; s < 8; ++s) kf[s] = *reinterpret_cast<const bf16x8*>(kslot + kro[s]);
    WAIT_LGKM0();
    if (it + 2 < n) issue(kbase, it + 2, wl_off + (it & 1) * 8192);
    f32x16 sc[2];
#pragma unroll
    for (int rb = 0; rb < 2; ++rb) {
#pragma unroll
      for (int r = 0; r < 16; ++r) sc[rb][r] = 0.f;
#pragma unroll
      for (int s = 0; s < 8; ++s) sc[rb] = __builtin_amdgcn_mfma_f32_32x32x16_bf16(kf[s], qf[rb][s], sc[rb], 0, 0, 0);
    }
    const int tok0 = (tile0 + wave + 4 * it) * kTile;
    if (tok0 + kTile > len) {
#pragma unroll
      for (int rb = 0; rb < 2; ++rb)
#pragma unroll
        for (int r = 0; r < 16; ++r) {
          const int tok = tok0 + (r & 3) + 8 * (r >> 2) + 4 * h;
          sc[rb][r] = tok < len ? sc[rb][r] : -INFINITY;
        }
    }
    bf16x8 pf[2][2];
    float alpha[2];
    bool need = false;
#pragma unroll
    for (int rb = 0; rb < 2; ++rb) {
      float mx = sc[rb][0];
#pragma unroll
      for (int r = 1; r < 16; ++r) mx = fmaxf(mx, sc[rb][r]);
      auto sw = __builtin_amdgcn_permlane32_swap(__float_as_uint(mx), __float_as_uint(mx), false, false);
      mx = fmaxf(__uint_as_float(sw[0]), __uint_as_float(sw[1])) * c2;
      const float mn = fmaxf(m[rb], mx);
      alpha[rb] = __builtin_amdgcn_exp2f(m[rb] - mn);
      need |= (mn > m[rb]);
      m[rb] = mn;
      float ps = 0.f;
#pragma unroll
      for (int r = 0; r < 16; ++r) {
        const float e = __builtin_amdgcn_exp2f(fmaf(sc[rb][r], c2, -mn));
        sc[rb][r] = e;
        ps += e;
      }
      l[rb] = fmaf(l[rb], alpha[rb], ps);
#pragma unroll
      for (int ks = 0; ks < 2; ++ks)
#pragma unroll
        for (int j = 0; j < 8; ++j) pf[rb][ks][j] = (__bf16)sc[rb][8 * ks + j];
    }
    if (__any(need)) {
#pragma unroll
      for (int d = 0; d < 4; ++d)
#pragma unroll
        for (int rb = 0; rb < 2; ++rb)
#pragma unroll
          for (int r = 0; r < 16; ++r) o[d][rb][r] *= alpha[rb];
    }
    if (it + 2 < n) WAIT_VM(24); else if (it + 1 < n) WAIT_VM(16); else WAIT_VM(0);
    bf16x8 vf[4][2];
#pragma unroll
    for (int d = 0; d < 4; ++d)
#pragma unroll
      for (int ks = 0; ks < 2; ++ks) {
        const bf16x4 lo = __builtin_amdgcn_ds_read_tr16_b64_v4bf16((lds_bf16x4_ptr)(vslot + vro[d][ks][0]));
        const bf16x4 hi = __builtin_amdgcn_ds_read_tr16_b64_v4bf16((lds_bf16x4_ptr)(vslot + vro[d][ks][1]));
        vf[d][ks] = __builtin_shufflevector(lo, hi, 0, 1, 2, 3, 4, 5, 6, 7);
      }
    WAIT_LGKM0();
    if (it + 2 < n) issue(vbase, it + 2, wl_off + 16384 + (it & 1) * 8192);
#pragma unroll
    for (int d = 0; d < 4; ++d)
#pragma unroll
      for (int ks = 0; ks < 2; ++ks)
#pragma unroll
        for (int rb = 0; rb < 2; ++rb)
          o[d][rb] = __builtin_amdgcn_mfma_f32_32x32x16_bf16(vf[d][ks], pf[rb][ks], o[d][rb], 0, 0, 0);
  }

  float* oimg = reinterpret_cast<float*>(wl);
  float* mlimg = reinterpret_cast<float*>(wl + 64 * kOPitch * 4);
#pragma unroll
  for (int rb = 0; rb < 2; ++rb) {
    auto sw = __builtin_amdgcn_permlane32_swap(__float_as_uint(l[rb]), __float_as_uint(l[rb]), false, false);
    const float lt = __uint_as_float(sw[0]) + __uint_as_float(sw[1]);
    if (h == 0) *reinterpret_cast<f32x2*>(mlimg + (32 * rb + r32) * 2) = f32x2{m[rb], lt};
#pragma unroll
    for (int d = 0; d < 4; ++d)
#pragma unroll
      for (int g = 0; g < 4; ++g)
        *reinterpret_cast<f32x4*>(oimg + (32 * rb + r32) * kOPitch + 32 * d + 8 * g + 4 * h) =
            f32x4{o[d][rb][4 * g], o[d][rb][4 * g + 1], o[d][rb][4 * g + 2], o[d][rb][4 * g + 3]};
  }
  __syncthreads();
  {
    const int d4 = (lane & 31) * 4;
#pragma unroll
    for (int ps = 0; ps < 8; ++ps) {
      const int row = 16 * wave + 2 * ps + (lane >> 5);
      float mw[4], lw[4];
      float M = -INFINITY;
#pragma unroll
      for (int w = 0; w < 4; ++w) {
        const f32x2 v = *reinterpret_cast<const f32x2*>(lds + w * kWaveLds + 64 * kOPitch * 4 + row * 8);
        mw[w] = v[0]; lw[w] = v[1];
        M = fmaxf(M, mw[w]);
      }
      f32x4 acc = {0.f, 0.f, 0.f, 0.f};
      float L = 0.f;
#pragma unroll
      for (int w = 0; w < 4; ++w) {
        const float sw = lw[w] > 0.f ? __builtin_amdgcn_exp2f(mw[w] - M) : 0.f;
        const f32x4 v = *reinterpret_cast<const f32x4*>(lds + w * kWaveLds + (row * kOPitch + d4) * 4);
        acc += sw * v;
        L = fmaf(sw, lw[w], L);
      }
      *reinterpret_cast<f32x4*>(po + ((size_t)blockIdx.x * kM + row) * kD + d4) = acc;
      if ((lane & 31) == 0) *reinterpret_cast<f32x2*>(pml + ((size_t)blockIdx.x * kM + row) * 2) = f32x2{M, L};
    }
  }
}

__global__ void __launch_bounds__(256)
attn_combine_kernel(const float* __restrict__ po, const float* __restrict__ pml, const int* __restrict__ cl,
                    __hip_bfloat16* __restrict__ out) {
  const int row = blockIdx.x * 8 + (threadIdx.x >> 5);
  const int d4 = (threadIdx.x & 31) * 4, lane = threadIdx.x & 63;
  const int b = (blockIdx.x * 8) / (kHQ * kSQ), rem = row % (kHQ * kSQ);
  const int kvh = rem / kM, R = rem % kM;
  Plan p;
  make_plan(cl, lane, p);
  const int li = p.row * 16 + b;
  const int ns = __builtin_amdgcn_readlane(p.ns, li);
  const int first = __builtin_amdgcn_readlane(p.sc, li) - ns;
  float M = -INFINITY;
  for (int j = 0; j < ns; ++j) M = fmaxf(M, pml[((size_t)((first + j) * kHKV + kvh) * kM + R) * 2]);
  float L = 0.f;
  f32x4 acc = {0.f, 0.f, 0.f, 0.f};
  for (int j = 0; j < ns; ++j) {
    const size_t idx = (size_t)((first + j) * kHKV + kvh) * kM + R;
    const float mj = pml[idx * 2], lj = pml[idx * 2 + 1];
    const float w = lj > 0.f ? __builtin_amdgcn_exp2f(mj - M) : 0.f;
    const f32x4 v = *reinterpret_cast<const f32x4*>(po + idx * kD + d4);
    acc += w * v;
    L = fmaf(w, lj, L);
  }
  const float inv = L > 0.f ? 1.f / L : 0.f;
  acc *= inv;
  bf16x4 r = {(__bf16)acc[0], (__bf16)acc[1], (__bf16)acc[2], (__bf16)acc[3]};
  *reinterpret_cast<bf16x4*>(reinterpret_cast<__bf16*>(out) + (size_t)row * kD + d4) = r;
}

}

extern "C" void kernel_launch(void* const* d_in, const int* in_sizes, int n_in, void* d_out, int out_size,
                              void* d_ws, size_t ws_size, hipStream_t stream) {
  const __hip_bfloat16* q = (const __hip_bfloat16*)d_in[0];
  const __hip_bfloat16* kc = (const __hip_bfloat16*)d_in[1];
  const __hip_bfloat16* vc = (const __hip_bfloat16*)d_in[2];
  const int* bt = (const int*)d_in[3];
  const int* cl = (const int*)d_in[4];
  float* po = (float*)d_ws;
  float* pml = po + (size_t)kNWG * kM * kD;
  (void)hipFuncSetAttribute((const void*)attn_split_kernel, hipFuncAttributeMaxDynamicSharedMemorySize, kLdsBytes);
  hipLaunchKernelGGL(attn_split_kernel, dim3(kNWG), dim3(256), kLdsBytes, stream, q, kc, vc, bt, cl, po, pml);
  hipLaunchKernelGGL(attn_combine_kernel, dim3(kB * kHQ * kSQ / 8), dim3(256), 0, stream, po, pml, cl,
                     (__hip_bfloat16*)d_out);
}
```

```cpp
#include <hip/hip_runtime.h>
#include <hip/hip_bf16.h>
#include <stdint.h>

namespace {

constexpr int kB = 16, kHQ = 32, kHKV = 4, kSQ = 8, kD = 128, kBS = 16, kNBPS = 256;
constexpr int kG = kHQ / kHKV;
constexpr int kM = kG * kSQ;
constexpr int kSKV = kNBPS * kBS;
constexpr int kTile = 32;
constexpr int kNWG = 256;
constexpr int kSlots = kNWG / kHKV;
constexpr int kWaveLds = 40960;
constexpr int kLdsBytes = 4 * kWaveLds;
constexpr int kOPitch = 132;

using bf16x8 = __attribute__((ext_vector_type(8))) __bf16;
using bf16x4 = __attribute__((ext_vector_type(4))) __bf16;
using bf16x2 = __attribute__((ext_vector_type(2))) __bf16;
using f32x16 = __attribute__((ext_vector_type(16))) float;
using f32x4 = __attribute__((ext_vector_type(4))) float;
using f32x2 = __attribute__((ext_vector_type(2))) float;
using u32x4 = __attribute__((ext_vector_type(4))) unsigned;
typedef __attribute__((address_space(3))) bf16x4* lds_bf16x4_ptr;

__device__ __forceinline__ unsigned off_b(unsigned row, unsigned ch) {
  return 256u * row + 16u * (ch ^ (((row & 3u) << 2) | ((row >> 2) & 3u)));
}

__device__ __forceinline__ int eff_len(int c) { return c <= 0 ? kSKV : (c > kSKV ? kSKV : c); }
__device__ __forceinline__ int row_scan(int v) {
  v += __builtin_amdgcn_update_dpp(0, v, 0x111, 0xf, 0xf, true);
  v += __builtin_amdgcn_update_dpp(0, v, 0x112, 0xf, 0xf, true);
  v += __builtin_amdgcn_update_dpp(0, v, 0x114, 0xf, 0xf, true);
  v += __builtin_amdgcn_update_dpp(0, v, 0x118, 0xf, 0xf, true);
  return v;
}
struct Plan {
  int nt, ns, sc, c;
  int row;
};
__device__ __forceinline__ void make_plan(const int* __restrict__ cl, int lane, Plan& p) {
  p.c = cl[lane & 15];
  p.nt = (eff_len(p.c) + kTile - 1) >> 5;
  const int total = __builtin_amdgcn_readlane(row_scan(p.nt), 15);
  int C0 = (total + kSlots - 1) / kSlots;
  for (;;) {
    const unsigned cand = (unsigned)(C0 + (lane >> 4));
    p.ns = (int)(((unsigned)p.nt + cand - 1u) / cand);
    p.sc = row_scan(p.ns);
    const unsigned long long ok = __ballot((lane & 15) == 15 && p.sc <= kSlots);
    if (ok) { p.row = (__ffsll((long long)ok) - 1) >> 4; break; }
    C0 += 4;
  }
}

#define STAMP(i) do {} while (0)
#define WAIT_VM(n) asm volatile("s_waitcnt vmcnt(" #n ")" ::: "memory")
#define WAIT_LGKM0() asm volatile("s_waitcnt lgkmcnt(0)" ::: "memory")

__global__ void __launch_bounds__(256, 1)
attn_split_kernel(const __hip_bfloat16* __restrict__ q, const __hip_bfloat16* __restrict__ kc,
                  const __hip_bfloat16* __restrict__ vc, const int* __restrict__ bt,
                  const int* __restrict__ cl, float* __restrict__ po, float* __restrict__ pml) {
  extern __shared__ __attribute__((aligned(16))) char lds[];
  const int tid = threadIdx.x, lane = tid & 63;
  const int wave = __builtin_amdgcn_readfirstlane(tid >> 6);
  const int r32 = lane & 31, h = lane >> 5;
  const int kvh = blockIdx.x & (kHKV - 1), slot = blockIdx.x / kHKV;

  STAMP(0);
  Plan p;
  make_plan(cl, lane, p);
  int b, tile0, tile1, clb;
  {
    const unsigned long long hit = __ballot((lane >> 4) == p.row && slot >= p.sc - p.ns && slot < p.sc);
    if (!hit) return;
    const int li = __ffsll((long long)hit) - 1;
    b = li & 15;
    const int nt = __builtin_amdgcn_readlane(p.nt, li), ns = __builtin_amdgcn_readlane(p.ns, li);
    const int j = slot - (__builtin_amdgcn_readlane(p.sc, li) - ns);
    clb = __builtin_amdgcn_readlane(p.c, li);
    const int base = nt / ns, rem = nt - base * ns;
    tile0 = j * base + (j < rem ? j : rem);
    tile1 = tile0 + base + (j < rem ? 1 : 0);
  }
  const int len = eff_len(clb);
  const float c2 = clb > 0 ? 0.08838834764831845f * 1.4426950408889634f : 0.f;

  STAMP(1);
  const int n = (tile1 - tile0 - wave + 3) >> 2;
  int btv = 0;
  {
    const int pg = 2 * (tile0 + wave + 4 * (lane >> 1)) + (lane & 1);
    if (pg < kNBPS) btv = bt[b * kNBPS + pg];
  }
  bf16x8 qf[2][8];
  {
    const __hip_bfloat16* qb = q + (size_t)(b * kHQ + kvh * kG) * kSQ * kD;
#pragma unroll
    for (int rb = 0; rb < 2; ++rb)
#pragma unroll
      for (int s = 0; s < 8; ++s)
        qf[rb][s] = *reinterpret_cast<const bf16x8*>(qb + (size_t)(32 * rb + r32) * kD + 16 * s + 8 * h);
  }

  unsigned voff[4];
#pragma unroll
  for (int i = 0; i < 4; ++i)
    voff[i] = (unsigned)(4 * i + (lane >> 4)) * 1024u + ((unsigned)((lane & 15) ^ (((lane >> 4) << 2) | i)) << 4);

  char* const wl = lds + wave * kWaveLds;
  const char* const kbase = reinterpret_cast<const char*>(kc) + kvh * 256;
  const char* const vbase = reinterpret_cast<const char*>(vc) + kvh * 256;

  auto issue = [&](const char* gb, int it, unsigned dst) {
    const unsigned pa = (unsigned)__builtin_amdgcn_readlane(btv, 2 * it);
    const unsigned pb = (unsigned)__builtin_amdgcn_readlane(btv, 2 * it + 1);
    const char* ga = gb + (size_t)pa * 16384u;
    const char* gbb = gb + (size_t)pb * 16384u;
    asm volatile(
        "s_nop 4\n\t"
        "s_mov_b32 m0, %0\n\ts_nop 0\n\tglobal_load_lds_dwordx4 %3, %1 nt\n\t"
        "s_add_u32 m0, m0, 1024\n\ts_nop 0\n\tglobal_load_lds_dwordx4 %4, %1 nt\n\t"
        "s_add_u32 m0, m0, 1024\n\ts_nop 0\n\tglobal_load_lds_dwordx4 %5, %1 nt\n\t"
        "s_add_u32 m0, m0, 1024\n\ts_nop 0\n\tglobal_load_lds_dwordx4 %6, %1 nt\n\t"
        "s_add_u32 m0, m0, 1024\n\ts_nop 0\n\tglobal_load_lds_dwordx4 %3, %2 nt\n\t"
        "s_add_u32 m0, m0, 1024\n\ts_nop 0\n\tglobal_load_lds_dwordx4 %4, %2 nt\n\t"
        "s_add_u32 m0, m0, 1024\n\ts_nop 0\n\tglobal_load_lds_dwordx4 %5, %2 nt\n\t"
        "s_add_u32 m0, m0, 1024\n\ts_nop 0\n\tglobal_load_lds_dwordx4 %6, %2 nt"
        :
        : "s"(dst), "s"(ga), "s"(gbb), "v"(voff[0]), "v"(voff[1]), "v"(voff[2]), "v"(voff[3])
        : "memory", "scc");
  };
  const unsigned wl_off = (unsigned)(uintptr_t)lds + (unsigned)wave * kWaveLds;

  f32x16 o[4][2];
#pragma unroll
  for (int d = 0; d < 4; ++d)
#pragma unroll
    for (int rb = 0; rb < 2; ++rb)
#pragma unroll
      for (int r = 0; r < 16; ++r) o[d][rb][r] = 0.f;
  float m[2] = {-INFINITY, -INFINITY}, l[2] = {0.f, 0.f};

#pragma unroll
  for (int rb = 0; rb < 2; ++rb)
#pragma unroll
    for (int s = 0; s < 8; ++s) asm volatile("" ::"v"(qf[rb][s]));
  asm volatile("" ::"v"(btv));
  STAMP(2);
  if (n > 0) { issue(kbase, 0, wl_off); issue(vbase, 0, wl_off + 16384); }
  if (n > 1) { issue(kbase, 1, wl_off + 8192); issue(vbase, 1, wl_off + 24576); }

  unsigned kro[8];
#pragma unroll
  for (int s = 0; s < 8; ++s) kro[s] = off_b(r32, 2 * s + h);
  unsigned vro[4][2][2];
  {
    const int blk = (lane >> 4) & 1, qq = (lane & 15) >> 2, pp = lane & 3;
#pragma unroll
    for (int d = 0; d < 4; ++d)
#pragma unroll
      for (int ks = 0; ks < 2; ++ks)
#pragma unroll
        for (int t = 0; t < 2; ++t)
          vro[d][ks][t] = off_b(16 * ks + 8 * t + 4 * h + qq, 4 * d + 2 * blk + (pp >> 1)) + 8 * (pp & 1);
  }

  for (int it = 0; it < n; ++it) {
    char* kslot = wl + (it & 1) * 8192;
    char* vslot = wl + 16384 + (it & 1) * 8192;
#pragma unroll
    for (int rb = 0; rb < 2; ++rb) {
#pragma unroll
      for (int d = 0; d < 4; ++d) asm volatile("" : "+a"(o[d][rb]));
#pragma unroll
      for (int s = 0; s < 8; ++s) asm volatile("" : "+a"(qf[rb][s]));
    }
    if (it + 1 < n) WAIT_VM(24); else WAIT_VM(8);
    if (it < 8) STAMP(3 + it);
    bf16x8 kf[8];
#pragma unroll
    for (int s = 0; s < 8; ++s) kf[s] = *reinterpret_cast<const bf16x8*>(kslot + kro[s]);
    WAIT_LGKM0();
    if (it + 2 < n) issue(kbase, it + 2, wl_off + (it & 1) * 8192);
    f32x16 sc[2];
#pragma unroll
    for (int rb = 0; rb < 2; ++rb) {
#pragma unroll
      for (int r = 0; r < 16; ++r) sc[rb][r] = 0.f;
#pragma unroll
      for (int s = 0; s < 8; ++s) sc[rb] = __builtin_amdgcn_mfma_f32_32x32x16_bf16(kf[s], qf[rb][s], sc[rb], 0, 0, 0);
    }
    const int tok0 = (tile0 + wave + 4 * it) * kTile;
    if (tok0 + kTile > len) {
#pragma unroll
      for (int rb = 0; rb < 2; ++rb)
#pragma unroll
        for (int r = 0; r < 16; ++r) {
          const int tok = tok0 + (r & 3) + 8 * (r >> 2) + 4 * h;
          sc[rb][r] = tok < len ? sc[rb][r] : -INFINITY;
        }
    }
    bf16x8 pf[2][2];
    float mx[2];
#pragma unroll
    for (int rb = 0; rb < 2; ++rb) {
      float t = sc[rb][0];
#pragma unroll
      for (int r = 1; r < 16; ++r) t = fmaxf(t, sc[rb][r]);
      auto sw = __builtin_amdgcn_permlane32_swap(__float_as_uint(t), __float_as_uint(t), false, false);
      mx[rb] = fmaxf(__uint_as_float(sw[0]), __uint_as_float(sw[1])) * c2;
    }
#pragma unroll
    for (int rb = 0; rb < 2; ++rb)
#pragma unroll
      for (int d = 0; d < 4; ++d) asm volatile("" : "+a"(o[d][rb]));
    if (__any(mx[0] > m[0] + 8.f || mx[1] > m[1] + 8.f)) {
#pragma unroll
      for (int rb = 0; rb < 2; ++rb) {
        const float mn = fmaxf(m[rb], mx[rb]);
        const float alpha = __builtin_amdgcn_exp2f(m[rb] - mn);
        m[rb] = mn;
        l[rb] *= alpha;
        if (it > 0) {
#pragma unroll
          for (int d = 0; d < 4; ++d)
#pragma unroll
            for (int r = 0; r < 16; ++r) o[d][rb][r] *= alpha;
        }
      }
    }
#pragma unroll
    for (int rb = 0; rb < 2; ++rb)
#pragma unroll
      for (int d = 0; d < 4; ++d) asm volatile("" : "+a"(o[d][rb]));
#pragma unroll
    for (int rb = 0; rb < 2; ++rb) {
      float ps = 0.f;
#pragma unroll
      for (int r = 0; r < 16; ++r) {
        const float e = __builtin_amdgcn_exp2f(fmaf(sc[rb][r], c2, -m[rb]));
        sc[rb][r] = e;
        ps += e;
      }
      l[rb] += ps;
#pragma unroll
      for (int ks = 0; ks < 2; ++ks)
#pragma unroll
        for (int j = 0; j < 8; ++j) pf[rb][ks][j] = (__bf16)sc[rb][8 * ks + j];
    }
    if (it + 2 < n) WAIT_VM(24); else if (it + 1 < n) WAIT_VM(16); else WAIT_VM(0);
    bf16x8 vf[4][2];
#pragma unroll
    for (int d = 0; d < 4; ++d)
#pragma unroll
      for (int ks = 0; ks < 2; ++ks) {
        const bf16x4 lo = __builtin_amdgcn_ds_read_tr16_b64_v4bf16((lds_bf16x4_ptr)(vslot + vro[d][ks][0]));
        const bf16x4 hi = __builtin_amdgcn_ds_read_tr16_b64_v4bf16((lds_bf16x4_ptr)(vslot + vro[d][ks][1]));
        vf[d][ks] = __builtin_shufflevector(lo, hi, 0, 1, 2, 3, 4, 5, 6, 7);
      }
    WAIT_LGKM0();
    if (it + 2 < n) issue(vbase, it + 2, wl_off + 16384 + (it & 1) * 8192);
#pragma unroll
    for (int d = 0; d < 4; ++d)
#pragma unroll
      for (int ks = 0; ks < 2; ++ks)
#pragma unroll
        for (int rb = 0; rb < 2; ++rb)
          o[d][rb] = __builtin_amdgcn_mfma_f32_32x32x16_bf16(vf[d][ks], pf[rb][ks], o[d][rb], 0, 0, 0);
  }

  STAMP(12);
  float* oimg = reinterpret_cast<float*>(wl);
  float* mlimg = reinterpret_cast<float*>(wl + 64 * kOPitch * 4);
#pragma unroll
  for (int rb = 0; rb < 2; ++rb) {
    auto sw = __builtin_amdgcn_permlane32_swap(__float_as_uint(l[rb]), __float_as_uint(l[rb]), false, false);
    const float lt = __uint_as_float(sw[0]) + __uint_as_float(sw[1]);
    if (h == 0) *reinterpret_cast<f32x2*>(mlimg + (32 * rb + r32) * 2) = f32x2{m[rb], lt};
#pragma unroll
    for (int d = 0; d < 4; ++d)
#pragma unroll
      for (int g = 0; g < 4; ++g)
        *reinterpret_cast<f32x4*>(oimg + (32 * rb + r32) * kOPitch + 32 * d + 8 * g + 4 * h) =
            f32x4{o[d][rb][4 * g], o[d][rb][4 * g + 1], o[d][rb][4 * g + 2], o[d][rb][4 * g + 3]};
  }
  __syncthreads();
  STAMP(13);
  {
    const int d4 = (lane & 31) * 4;
#pragma unroll
    for (int ps = 0; ps < 8; ++ps) {
      const int row = 16 * wave + 2 * ps + (lane >> 5);
      float mw[4], lw[4];
      float M = -INFINITY;
#pragma unroll
      for (int w = 0; w < 4; ++w) {
        const f32x2 v = *reinterpret_cast<const f32x2*>(lds + w * kWaveLds + 64 * kOPitch * 4 + row * 8);
        mw[w] = v[0]; lw[w] = v[1];
        M = fmaxf(M, mw[w]);
      }
      f32x4 acc = {0.f, 0.f, 0.f, 0.f};
      float L = 0.f;
#pragma unroll
      for (int w = 0; w < 4; ++w) {
        const float sw = lw[w] > 0.f ? __builtin_amdgcn_exp2f(mw[w] - M) : 0.f;
        const f32x4 v = *reinterpret_cast<const f32x4*>(lds + w * kWaveLds + (row * kOPitch + d4) * 4);
        acc += sw * v;
        L = fmaf(sw, lw[w], L);
      }
      *reinterpret_cast<f32x4*>(po + ((size_t)blockIdx.x * kM + row) * kD + d4) = acc;
      if ((lane & 31) == 0) *reinterpret_cast<f32x2*>(pml + ((size_t)blockIdx.x * kM + row) * 2) = f32x2{M, L};
    }
  }
  STAMP(14);
}

__global__ void __launch_bounds__(256)
attn_combine_kernel(const float* __restrict__ po, const float* __restrict__ pml, const int* __restrict__ cl,
                    __hip_bfloat16* __restrict__ out) {
  const int row = blockIdx.x * 8 + (threadIdx.x >> 5);
  const int d4 = (threadIdx.x & 31) * 4, lane = threadIdx.x & 63;
  const int b = (blockIdx.x * 8) / (kHQ * kSQ), rem = row % (kHQ * kSQ);
  const int kvh = rem / kM, R = rem % kM;
  Plan p;
  make_plan(cl, lane, p);
  const int li = p.row * 16 + b;
  const int ns = __builtin_amdgcn_readlane(p.ns, li);
  const int first = __builtin_amdgcn_readlane(p.sc, li) - ns;
  float M = -INFINITY;
  for (int j = 0; j < ns; ++j) M = fmaxf(M, pml[((size_t)((first + j) * kHKV + kvh) * kM + R) * 2]);
  float L = 0.f;
  f32x4 acc = {0.f, 0.f, 0.f, 0.f};
  for (int j = 0; j < ns; ++j) {
    const size_t idx = (size_t)((first + j) * kHKV + kvh) * kM + R;
    const float mj = pml[idx * 2], lj = pml[idx * 2 + 1];
    const float w = lj > 0.f ? __builtin_amdgcn_exp2f(mj - M) : 0.f;
    const f32x4 v = *reinterpret_cast<const f32x4*>(po + idx * kD + d4);
    acc += w * v;
    L = fmaf(w, lj, L);
  }
  const float inv = L > 0.f ? 1.f / L : 0.f;
  acc *= inv;
  bf16x4 r = {(__bf16)acc[0], (__bf16)acc[1], (__bf16)acc[2], (__bf16)acc[3]};
  *reinterpret_cast<bf16x4*>(reinterpret_cast<__bf16*>(out) + (size_t)row * kD + d4) = r;
}

}

extern "C" void kernel_launch(void* const* d_in, const int* in_sizes, int n_in, void* d_out, int out_size,
                              void* d_ws, size_t ws_size, hipStream_t stream) {
  const __hip_bfloat16* q = (const __hip_bfloat16*)d_in[0];
  const __hip_bfloat16* kc = (const __hip_bfloat16*)d_in[1];
  const __hip_bfloat16* vc = (const __hip_bfloat16*)d_in[2];
  const int* bt = (const int*)d_in[3];
  const int* cl = (const int*)d_in[4];
  float* po = (float*)d_ws;
  float* pml = po + (size_t)kNWG * kM * kD;
  (void)hipFuncSetAttribute((const void*)attn_split_kernel, hipFuncAttributeMaxDynamicSharedMemorySize, kLdsBytes);
  hipLaunchKernelGGL(attn_split_kernel, dim3(kNWG), dim3(256), kLdsBytes, stream, q, kc, vc, bt, cl, po, pml);
  hipLaunchKernelGGL(attn_combine_kernel, dim3(kB * kHQ * kSQ / 8), dim3(256), 0, stream, po, pml, cl,
                     (__hip_bfloat16*)d_out);
}
```

```cpp
#include <hip/hip_runtime.h>
#include <hip/hip_bf16.h>
#include <stdint.h>

namespace {

constexpr int kB = 16, kHQ = 32, kHKV = 4, kSQ = 8, kD = 128, kBS = 16, kNBPS = 256;
constexpr int kG = kHQ / kHKV;
constexpr int kM = kG * kSQ;
constexpr int kSKV = kNBPS * kBS;
constexpr int kTile = 32;
constexpr int kNWG = 256;
constexpr int kSlots = kNWG / kHKV;
constexpr int kWaveLds = 40960;
constexpr int kLdsBytes = 4 * kWaveLds;
constexpr int kOPitch = 132;

using bf16x8 = __attribute__((ext_vector_type(8))) __bf16;
using bf16x4 = __attribute__((ext_vector_type(4))) __bf16;
using bf16x2 = __attribute__((ext_vector_type(2))) __bf16;
using f32x16 = __attribute__((ext_vector_type(16))) float;
using f32x4 = __attribute__((ext_vector_type(4))) float;
using f32x2 = __attribute__((ext_vector_type(2))) float;
using u32x4 = __attribute__((ext_vector_type(4))) unsigned;
typedef __attribute__((address_space(3))) bf16x4* lds_bf16x4_ptr;

__device__ __forceinline__ unsigned off_b(unsigned row, unsigned ch) {
  return 256u * row + 16u * (ch ^ (((row & 3u) << 2) | ((row >> 2) & 3u)));
}

__device__ __forceinline__ int eff_len(int c) { return c <= 0 ? kSKV : (c > kSKV ? kSKV : c); }
__device__ __forceinline__ int row_scan(int v) {
  v += __builtin_amdgcn_update_dpp(0, v, 0x111, 0xf, 0xf, true);
  v += __builtin_amdgcn_update_dpp(0, v, 0x112, 0xf, 0xf, true);
  v += __builtin_amdgcn_update_dpp(0, v, 0x114, 0xf, 0xf, true);
  v += __builtin_amdgcn_update_dpp(0, v, 0x118, 0xf, 0xf, true);
  return v;
}
struct Plan {
  int nt, ns, sc, c;
  int row;
};
__device__ __forceinline__ void make_plan(const int* __restrict__ cl, int lane, Plan& p) {
  p.c = cl[lane & 15];
  p.nt = (eff_len(p.c) + kTile - 1) >> 5;
  const int total = __builtin_amdgcn_readlane(row_scan(p.nt), 15);
  int C0 = (total + kSlots - 1) / kSlots;
  for (;;) {
    const unsigned cand = (unsigned)(C0 + (lane >> 4));
    p.ns = (int)(((unsigned)p.nt + cand - 1u) / cand);
    p.sc = row_scan(p.ns);
    const unsigned long long ok = __ballot((lane & 15) == 15 && p.sc <= kSlots);
    if (ok) { p.row = (__ffsll((long long)ok) - 1) >> 4; break; }
    C0 += 4;
  }
}

#define STAMP(i) do {} while (0)
#define WAIT_VM(n) asm volatile("s_waitcnt vmcnt(" #n ")" ::: "memory")
#define WAIT_LGKM0() asm volatile("s_waitcnt lgkmcnt(0)" ::: "memory")

__global__ void __launch_bounds__(256, 1)
attn_split_kernel(const __hip_bfloat16* __restrict__ q, const __hip_bfloat16* __restrict__ kc,
                  const __hip_bfloat16* __restrict__ vc, const int* __restrict__ bt,
                  const int* __restrict__ cl, float* __restrict__ po, float* __restrict__ pml) {
  extern __shared__ __attribute__((aligned(16))) char lds[];
  const int tid = threadIdx.x, lane = tid & 63;
  const int wave = __builtin_amdgcn_readfirstlane(tid >> 6);
  const int r32 = lane & 31, h = lane >> 5;
  const int kvh = blockIdx.x & (kHKV - 1), slot = blockIdx.x / kHKV;

  STAMP(0);
  {
    const char* src = reinterpret_cast<const char*>(bt) + wave * 4096;
    const unsigned dst = (unsigned)(uintptr_t)lds + (unsigned)wave * kWaveLds + 32768u;
    asm volatile(
        "s_mov_b32 m0, %0\n\ts_nop 0\n\tglobal_load_lds_dwordx4 %2, %1\n\t"
        "global_load_lds_dwordx4 %2, %1 offset:1024\n\t"
        "global_load_lds_dwordx4 %2, %1 offset:2048\n\t"
        "global_load_lds_dwordx4 %2, %1 offset:3072"
        :
        : "s"(dst), "s"(src), "v"((unsigned)lane * 16u)
        : "memory", "scc");
  }
  Plan p;
  make_plan(cl, lane, p);
  int b, tile0, tile1, clb;
  bool active;
  {
    const unsigned long long hit = __ballot((lane >> 4) == p.row && slot >= p.sc - p.ns && slot < p.sc);
    active = hit != 0;
    const int li = active ? __ffsll((long long)hit) - 1 : 0;
    b = li & 15;
    const int nt = __builtin_amdgcn_readlane(p.nt, li), ns = __builtin_amdgcn_readlane(p.ns, li);
    const int j = slot - (__builtin_amdgcn_readlane(p.sc, li) - ns);
    clb = __builtin_amdgcn_readlane(p.c, li);
    const int base = nt / ns, rem = nt - base * ns;
    tile0 = j * base + (j < rem ? j : rem);
    tile1 = tile0 + base + (j < rem ? 1 : 0);
  }
  WAIT_VM(0);
  __syncthreads();
  if (!active) return;
  const int len = eff_len(clb);
  const float c2 = clb > 0 ? 0.08838834764831845f * 1.4426950408889634f : 0.f;

  STAMP(1);
  const int n = (tile1 - tile0 - wave + 3) >> 2;
  int btv = 0;
  {
    const int pg = 2 * (tile0 + wave + 4 * (lane >> 1)) + (lane & 1);
    if (pg < kNBPS) btv = *reinterpret_cast<const int*>(lds + (b >> 2) * kWaveLds + 32768 + (b & 3) * 1024 + pg * 4);
  }
  bf16x8 qf[2][8];
  {
    const char* qb = reinterpret_cast<const char*>(q + (size_t)(b * kHQ + kvh * kG) * kSQ * kD);
    const unsigned qo = (unsigned)r32 * 256u + 16u * (unsigned)h;
#pragma unroll
    for (int rb = 0; rb < 2; ++rb)
#pragma unroll
      for (int s = 0; s < 8; ++s)
        asm volatile("global_load_dwordx4 %0, %1, %2 offset:%3" : "=a"(qf[rb][s]) : "v"(qo), "s"(qb + rb * 8192), "i"(32 * s));
  }

  unsigned voff[4];
#pragma unroll
  for (int i = 0; i < 4; ++i)
    voff[i] = (unsigned)(4 * i + (lane >> 4)) * 1024u + ((unsigned)((lane & 15) ^ (((lane >> 4) << 2) | i)) << 4);

  char* const wl = lds + wave * kWaveLds;
  const char* const kbase = reinterpret_cast<const char*>(kc) + kvh * 256;
  const char* const vbase = reinterpret_cast<const char*>(vc) + kvh * 256;

  auto issue = [&](const char* gb, int it, unsigned dst) {
    const unsigned pa = (unsigned)__builtin_amdgcn_readlane(btv, 2 * it);
    const unsigned pb = (unsigned)__builtin_amdgcn_readlane(btv, 2 * it + 1);
    const char* ga = gb + (size_t)pa * 16384u;
    const char* gbb = gb + (size_t)pb * 16384u;
    asm volatile(
        "s_nop 4\n\t"
        "s_mov_b32 m0, %0\n\ts_nop 0\n\tglobal_load_lds_dwordx4 %3, %1 nt\n\t"
        "s_add_u32 m0, m0, 1024\n\ts_nop 0\n\tglobal_load_lds_dwordx4 %4, %1 nt\n\t"
        "s_add_u32 m0, m0, 1024\n\ts_nop 0\n\tglobal_load_lds_dwordx4 %5, %1 nt\n\t"
        "s_add_u32 m0, m0, 1024\n\ts_nop 0\n\tglobal_load_lds_dwordx4 %6, %1 nt\n\t"
        "s_add_u32 m0, m0, 1024\n\ts_nop 0\n\tglobal_load_lds_dwordx4 %3, %2 nt\n\t"
        "s_add_u32 m0, m0, 1024\n\ts_nop 0\n\tglobal_load_lds_dwordx4 %4, %2 nt\n\t"
        "s_add_u32 m0, m0, 1024\n\ts_nop 0\n\tglobal_load_lds_dwordx4 %5, %2 nt\n\t"
        "s_add_u32 m0, m0, 1024\n\ts_nop 0\n\tglobal_load_lds_dwordx4 %6, %2 nt"
        :
        : "s"(dst), "s"(ga), "s"(gbb), "v"(voff[0]), "v"(voff[1]), "v"(voff[2]), "v"(voff[3])
        : "memory", "scc");
  };
  const unsigned wl_off = (unsigned)(uintptr_t)lds + (unsigned)wave * kWaveLds;

  f32x16 o[4][2];
#pragma unroll
  for (int d = 0; d < 4; ++d)
#pragma unroll
    for (int rb = 0; rb < 2; ++rb)
#pragma unroll
      for (int r = 0; r < 16; ++r) o[d][rb][r] = 0.f;
  float m[2] = {-INFINITY, -INFINITY}, l[2] = {0.f, 0.f};

  STAMP(2);
  if (n > 0) { issue(kbase, 0, wl_off); issue(vbase, 0, wl_off + 16384); }
  if (n > 1) { issue(kbase, 1, wl_off + 8192); issue(vbase, 1, wl_off + 24576); }

  unsigned kro[8];
#pragma unroll
  for (int s = 0; s < 8; ++s) kro[s] = off_b(r32, 2 * s + h);
  unsigned vro[4][2][2];
  {
    const int blk = (lane >> 4) & 1, qq = (lane & 15) >> 2, pp = lane & 3;
#pragma unroll
    for (int d = 0; d < 4; ++d)
#pragma unroll
      for (int ks = 0; ks < 2; ++ks)
#pragma unroll
        for (int t = 0; t < 2; ++t)
          vro[d][ks][t] = off_b(16 * ks + 8 * t + 4 * h + qq, 4 * d + 2 * blk + (pp >> 1)) + 8 * (pp & 1);
  }

  for (int it = 0; it < n; ++it) {
    char* kslot = wl + (it & 1) * 8192;
    char* vslot = wl + 16384 + (it & 1) * 8192;
#pragma unroll
    for (int rb = 0; rb < 2; ++rb) {
#pragma unroll
      for (int d = 0; d < 4; ++d) asm volatile("" : "+a"(o[d][rb]));
#pragma unroll
      for (int s = 0; s < 8; ++s) asm volatile("" : "+a"(qf[rb][s]));
    }
    if (it + 1 < n) WAIT_VM(24); else WAIT_VM(8);
    if (it < 8) STAMP(3 + it);
    bf16x8 kf[8];
#pragma unroll
    for (int s = 0; s < 8; ++s) kf[s] = *reinterpret_cast<const bf16x8*>(kslot + kro[s]);
    WAIT_LGKM0();
    if (it + 2 < n) issue(kbase, it + 2, wl_off + (it & 1) * 8192);
    f32x16 sc[2];
#pragma unroll
    for (int rb = 0; rb < 2; ++rb) {
#pragma unroll
      for (int r = 0; r < 16; ++r) sc[rb][r] = 0.f;
#pragma unroll
      for (int s = 0; s < 8; ++s) sc[rb] = __builtin_amdgcn_mfma_f32_32x32x16_bf16(kf[s], qf[rb][s], sc[rb], 0, 0, 0);
    }
    const int tok0 = (tile0 + wave + 4 * it) * kTile;
    if (tok0 + kTile > len) {
#pragma unroll
      for (int rb = 0; rb < 2; ++rb)
#pragma unroll
        for (int r = 0; r < 16; ++r) {
          const int tok = tok0 + (r & 3) + 8 * (r >> 2) + 4 * h;
          sc[rb][r] = tok < len ? sc[rb][r] : -INFINITY;
        }
    }
    bf16x8 pf[2][2];
    float mx[2];
#pragma unroll
    for (int rb = 0; rb < 2; ++rb) {
      float t = sc[rb][0];
#pragma unroll
      for (int r = 1; r < 16; ++r) t = fmaxf(t, sc[rb][r]);
      auto sw = __builtin_amdgcn_permlane32_swap(__float_as_uint(t), __float_as_uint(t), false, false);
      mx[rb] = fmaxf(__uint_as_float(sw[0]), __uint_as_float(sw[1])) * c2;
    }
#pragma unroll
    for (int rb = 0; rb < 2; ++rb)
#pragma unroll
      for (int d = 0; d < 4; ++d) asm volatile("" : "+a"(o[d][rb]));
    if (__any(mx[0] > m[0] + 8.f || mx[1] > m[1] + 8.f)) {
#pragma unroll
      for (int rb = 0; rb < 2; ++rb) {
        const float mn = fmaxf(m[rb], mx[rb]);
        const float alpha = __builtin_amdgcn_exp2f(m[rb] - mn);
        m[rb] = mn;
        l[rb] *= alpha;
        if (it > 0) {
#pragma unroll
          for (int d = 0; d < 4; ++d)
#pragma unroll
            for (int r = 0; r < 16; ++r) o[d][rb][r] *= alpha;
        }
      }
    }
#pragma unroll
    for (int rb = 0; rb < 2; ++rb)
#pragma unroll
      for (int d = 0; d < 4; ++d) asm volatile("" : "+a"(o[d][rb]));
#pragma unroll
    for (int rb = 0; rb < 2; ++rb) {
      float ps = 0.f;
#pragma unroll
      for (int r = 0; r < 16; ++r) {
        const float e = __builtin_amdgcn_exp2f(fmaf(sc[rb][r], c2, -m[rb]));
        sc[rb][r] = e;
        ps += e;
      }
      l[rb] += ps;
#pragma unroll
      for (int ks = 0; ks < 2; ++ks)
#pragma unroll
        for (int j = 0; j < 8; ++j) pf[rb][ks][j] = (__bf16)sc[rb][8 * ks + j];
    }
    if (it + 2 < n) WAIT_VM(24); else if (it + 1 < n) WAIT_VM(16); else WAIT_VM(0);
    bf16x8 vf[4][2];
#pragma unroll
    for (int d = 0; d < 4; ++d)
#pragma unroll
      for (int ks = 0; ks < 2; ++ks) {
        const bf16x4 lo = __builtin_amdgcn_ds_read_tr16_b64_v4bf16((lds_bf16x4_ptr)(vslot + vro[d][ks][0]));
        const bf16x4 hi = __builtin_amdgcn_ds_read_tr16_b64_v4bf16((lds_bf16x4_ptr)(vslot + vro[d][ks][1]));
        vf[d][ks] = __builtin_shufflevector(lo, hi, 0, 1, 2, 3, 4, 5, 6, 7);
      }
    WAIT_LGKM0();
    if (it + 2 < n) issue(vbase, it + 2, wl_off + 16384 + (it & 1) * 8192);
#pragma unroll
    for (int d = 0; d < 4; ++d)
#pragma unroll
      for (int ks = 0; ks < 2; ++ks)
#pragma unroll
        for (int rb = 0; rb < 2; ++rb)
          o[d][rb] = __builtin_amdgcn_mfma_f32_32x32x16_bf16(vf[d][ks], pf[rb][ks], o[d][rb], 0, 0, 0);
  }

  WAIT_VM(0);
  STAMP(12);
  float* oimg = reinterpret_cast<float*>(wl);
  float* mlimg = reinterpret_cast<float*>(wl + 64 * kOPitch * 4);
#pragma unroll
  for (int rb = 0; rb < 2; ++rb) {
    auto sw = __builtin_amdgcn_permlane32_swap(__float_as_uint(l[rb]), __float_as_uint(l[rb]), false, false);
    const float lt = __uint_as_float(sw[0]) + __uint_as_float(sw[1]);
    if (h == 0) *reinterpret_cast<f32x2*>(mlimg + (32 * rb + r32) * 2) = f32x2{m[rb], lt};
#pragma unroll
    for (int d = 0; d < 4; ++d)
#pragma unroll
      for (int g = 0; g < 4; ++g)
        *reinterpret_cast<f32x4*>(oimg + (32 * rb + r32) * kOPitch + 32 * d + 8 * g + 4 * h) =
            f32x4{o[d][rb][4 * g], o[d][rb][4 * g + 1], o[d][rb][4 * g + 2], o[d][rb][4 * g + 3]};
  }
  __syncthreads();
  STAMP(13);
  {
    const int d4 = (lane & 31) * 4;
#pragma unroll
    for (int ps = 0; ps < 8; ++ps) {
      const int row = 16 * wave + 2 * ps + (lane >> 5);
      float mw[4], lw[4];
      float M = -INFINITY;
#pragma unroll
      for (int w = 0; w < 4; ++w) {
        const f32x2 v = *reinterpret_cast<const f32x2*>(lds + w * kWaveLds + 64 * kOPitch * 4 + row * 8);
        mw[w] = v[0]; lw[w] = v[1];
        M = fmaxf(M, mw[w]);
      }
      f32x4 acc = {0.f, 0.f, 0.f, 0.f};
      float L = 0.f;
#pragma unroll
      for (int w = 0; w < 4; ++w) {
        const float sw = lw[w] > 0.f ? __builtin_amdgcn_exp2f(mw[w] - M) : 0.f;
        const f32x4 v = *reinterpret_cast<const f32x4*>(lds + w * kWaveLds + (row * kOPitch + d4) * 4);
        acc += sw * v;
        L = fmaf(sw, lw[w], L);
      }
      *reinterpret_cast<f32x4*>(po + ((size_t)blockIdx.x * kM + row) * kD + d4) = acc;
      if ((lane & 31) == 0) *reinterpret_cast<f32x2*>(pml + ((size_t)blockIdx.x * kM + row) * 2) = f32x2{M, L};
    }
  }
  STAMP(14);
}

__global__ void __launch_bounds__(256)
attn_combine_kernel(const float* __restrict__ po, const float* __restrict__ pml, const int* __restrict__ cl,
                    __hip_bfloat16* __restrict__ out) {
  const int row = blockIdx.x * 8 + (threadIdx.x >> 5);
  const int d4 = (threadIdx.x & 31) * 4, lane = threadIdx.x & 63;
  const int b = (blockIdx.x * 8) / (kHQ * kSQ), rem = row % (kHQ * kSQ);
  const int kvh = rem / kM, R = rem % kM;
  Plan p;
  make_plan(cl, lane, p);
  const int li = p.row * 16 + b;
  const int ns = __builtin_amdgcn_readlane(p.ns, li);
  const int first = __builtin_amdgcn_readlane(p.sc, li) - ns;
  float M = -INFINITY, L = 0.f;
  f32x4 acc = {0.f, 0.f, 0.f, 0.f};
  for (int j0 = 0; j0 < ns; j0 += 4) {
    f32x2 ml[4];
    f32x4 v[4];
#pragma unroll
    for (int k = 0; k < 4; ++k) {
      const int j = j0 + k < ns ? j0 + k : j0;
      const size_t idx = (size_t)((first + j) * kHKV + kvh) * kM + R;
      ml[k] = *reinterpret_cast<const f32x2*>(pml + idx * 2);
      v[k] = *reinterpret_cast<const f32x4*>(po + idx * kD + d4);
    }
    float Mn = M;
#pragma unroll
    for (int k = 0; k < 4; ++k) {
      if (j0 + k >= ns) ml[k] = f32x2{-INFINITY, 0.f};
      Mn = fmaxf(Mn, ml[k][0]);
    }
    const float sc = __builtin_amdgcn_exp2f(M - Mn);
    acc *= sc;
    L *= sc;
#pragma unroll
    for (int k = 0; k < 4; ++k) {
      const float w = ml[k][1] > 0.f ? __builtin_amdgcn_exp2f(ml[k][0] - Mn) : 0.f;
      acc += w * v[k];
      L = fmaf(w, ml[k][1], L);
    }
    M = Mn;
  }
  const float inv = L > 0.f ? 1.f / L : 0.f;
  acc *= inv;
  bf16x4 r = {(__bf16)acc[0], (__bf16)acc[1], (__bf16)acc[2], (__bf16)acc[3]};
  *reinterpret_cast<bf16x4*>(reinterpret_cast<__bf16*>(out) + (size_t)row * kD + d4) = r;
}

}

extern "C" void kernel_launch(void* const* d_in, const int* in_sizes, int n_in, void* d_out, int out_size,
                              void* d_ws, size_t ws_size, hipStream_t stream) {
  const __hip_bfloat16* q = (const __hip_bfloat16*)d_in[0];
  const __hip_bfloat16* kc = (const __hip_bfloat16*)d_in[1];
  const __hip_bfloat16* vc = (const __hip_bfloat16*)d_in[2];
  const int* bt = (const int*)d_in[3];
  const int* cl = (const int*)d_in[4];
  float* po = (float*)d_ws;
  float* pml = po + (size_t)kNWG * kM * kD;
  (void)hipFuncSetAttribute((const void*)attn_split_kernel, hipFuncAttributeMaxDynamicSharedMemorySize, kLdsBytes);
  hipLaunchKernelGGL(attn_split_kernel, dim3(kNWG), dim3(256), kLdsBytes, stream, q, kc, vc, bt, cl, po, pml);
  hipLaunchKernelGGL(attn_combine_kernel, dim3(kB * kHQ * kSQ / 8), dim3(256), 0, stream, po, pml, cl,
                     (__hip_bfloat16*)d_out);
}
```

```cpp
#include <hip/hip_runtime.h>
#include <hip/hip_bf16.h>
#include <stdint.h>

namespace {

constexpr int kB = 16, kHQ = 32, kHKV = 4, kSQ = 8, kD = 128, kBS = 16, kNBPS = 256;
constexpr int kG = kHQ / kHKV;
constexpr int kM = kG * kSQ;
constexpr int kSKV = kNBPS * kBS;
constexpr int kTile = 32;
constexpr int kNWG = 256;
constexpr int kSlots = kNWG;
constexpr int kWaveLds = 40960;
constexpr int kLdsBytes = 4 * kWaveLds;
constexpr int kOPitch = 132;

using bf16x8 = __attribute__((ext_vector_type(8))) __bf16;
using bf16x4 = __attribute__((ext_vector_type(4))) __bf16;
using bf16x2 = __attribute__((ext_vector_type(2))) __bf16;
using f32x16 = __attribute__((ext_vector_type(16))) float;
using f32x4 = __attribute__((ext_vector_type(4))) float;
using f32x2 = __attribute__((ext_vector_type(2))) float;
using u32x4 = __attribute__((ext_vector_type(4))) unsigned;
typedef __attribute__((address_space(3))) bf16x4* lds_bf16x4_ptr;

__device__ __forceinline__ unsigned off_b(unsigned row, unsigned ch) {
  return 256u * row + 16u * (ch ^ (((row & 3u) << 2) | ((row >> 2) & 3u)));
}

__device__ __forceinline__ int eff_len(int c) { return c <= 0 ? kSKV : (c > kSKV ? kSKV : c); }
__device__ __forceinline__ int row_scan(int v) {
  v += __builtin_amdgcn_update_dpp(0, v, 0x111, 0xf, 0xf, true);
  v += __builtin_amdgcn_update_dpp(0, v, 0x112, 0xf, 0xf, true);
  v += __builtin_amdgcn_update_dpp(0, v, 0x114, 0xf, 0xf, true);
  v += __builtin_amdgcn_update_dpp(0, v, 0x118, 0xf, 0xf, true);
  return v;
}
struct Plan {
  int nt, ns, sc, c;
  int row;
};
__device__ __forceinline__ void make_plan(const int* __restrict__ cl, int lane, Plan& p) {
  p.c = cl[lane & 15];
  p.nt = (eff_len(p.c) + kBS - 1) >> 4;
  const int total = __builtin_amdgcn_readlane(row_scan(p.nt), 15);
  int C0 = (total + kSlots - 1) / kSlots;
  for (;;) {
    const unsigned cand = (unsigned)(C0 + (lane >> 4));
    p.ns = (int)(((unsigned)p.nt + cand - 1u) / cand);
    p.sc = row_scan(p.ns);
    const unsigned long long ok = __ballot((lane & 15) == 15 && p.sc <= kSlots);
    if (ok) { p.row = (__ffsll((long long)ok) - 1) >> 4; break; }
    C0 += 4;
  }
}

#define STAMP(i) do {} while (0)
#define WAIT_VM(n) asm volatile("s_waitcnt vmcnt(" #n ")" ::: "memory")
#define WAIT_LGKM0() asm volatile("s_waitcnt lgkmcnt(0)" ::: "memory")

__global__ void __launch_bounds__(256, 1)
attn_split_kernel(const __hip_bfloat16* __restrict__ q, const __hip_bfloat16* __restrict__ kc,
                  const __hip_bfloat16* __restrict__ vc, const int* __restrict__ bt,
                  const int* __restrict__ cl, float* __restrict__ po, float* __restrict__ pml) {
  extern __shared__ __attribute__((aligned(16))) char lds[];
  const int tid = threadIdx.x, lane = tid & 63;
  const int wave = __builtin_amdgcn_readfirstlane(tid >> 6);
  const int r32 = lane & 31, h = lane >> 5;
  const int kvh = blockIdx.x & (kHKV - 1), slot = blockIdx.x / kHKV;

  STAMP(0);
  {
    const char* src = reinterpret_cast<const char*>(bt) + wave * 4096;
    const unsigned dst = (unsigned)(uintptr_t)lds + (unsigned)wave * kWaveLds + 32768u;
    asm volatile(
        "s_mov_b32 m0, %0\n\ts_nop 0\n\tglobal_load_lds_dwordx4 %2, %1\n\t"
        "global_load_lds_dwordx4 %2, %1 offset:1024\n\t"
        "global_load_lds_dwordx4 %2, %1 offset:2048\n\t"
        "global_load_lds_dwordx4 %2, %1 offset:3072"
        :
        : "s"(dst), "s"(src), "v"((unsigned)lane * 16u)
        : "memory", "scc");
  }
  Plan p;
  make_plan(cl, lane, p);
  int b, p0, p1, clb;
  bool active;
  {
    const int g = slot * 4 + wave;
    const unsigned long long hit = __ballot((lane >> 4) == p.row && g >= p.sc - p.ns && g < p.sc);
    active = hit != 0;
    const int li = active ? __ffsll((long long)hit) - 1 : 0;
    b = li & 15;
    const int np = __builtin_amdgcn_readlane(p.nt, li), ns = __builtin_amdgcn_readlane(p.ns, li);
    const int j = g - (__builtin_amdgcn_readlane(p.sc, li) - ns);
    clb = __builtin_amdgcn_readlane(p.c, li);
    const int base = np / ns, rem = np - base * ns;
    p0 = j * base + (j < rem ? j : rem);
    p1 = p0 + base + (j < rem ? 1 : 0);
  }
  WAIT_VM(0);
  __syncthreads();
  if (!active) { p0 = p1 = 0; b = 0; }
  const int len = eff_len(clb);
  const int lim = len < p1 * kBS ? len : p1 * kBS;
  const float c2 = clb > 0 ? 0.08838834764831845f * 1.4426950408889634f : 0.f;

  STAMP(1);
  const int n = (p1 - p0 + 1) >> 1;
  int btv = 0;
  {
    const int pg = p0 + (p0 + lane < p1 ? lane : (p1 - p0 - 1));
    if (active) btv = *reinterpret_cast<const int*>(lds + (b >> 2) * kWaveLds + 32768 + (b & 3) * 1024 + pg * 4);
  }
  bf16x8 qf[2][8];
  {
    const char* qb = reinterpret_cast<const char*>(q + (size_t)(b * kHQ + kvh * kG) * kSQ * kD);
    const unsigned qo = (unsigned)r32 * 256u + 16u * (unsigned)h;
#pragma unroll
    for (int rb = 0; rb < 2; ++rb)
#pragma unroll
      for (int s = 0; s < 8; ++s)
        asm volatile("global_load_dwordx4 %0, %1, %2 offset:%3" : "=a"(qf[rb][s]) : "v"(qo), "s"(qb + rb * 8192), "i"(32 * s));
  }

  unsigned voff[4];
#pragma unroll
  for (int i = 0; i < 4; ++i)
    voff[i] = (unsigned)(4 * i + (lane >> 4)) * 1024u + ((unsigned)((lane & 15) ^ (((lane >> 4) << 2) | i)) << 4);

  char* const wl = lds + wave * kWaveLds;
  const char* const kbase = reinterpret_cast<const char*>(kc) + kvh * 256;
  const char* const vbase = reinterpret_cast<const char*>(vc) + kvh * 256;

  auto issue = [&](const char* gb, int it, unsigned dst) {
    const unsigned pa = (unsigned)__builtin_amdgcn_readlane(btv, 2 * it);
    const unsigned pb = (unsigned)__builtin_amdgcn_readlane(btv, 2 * it + 1);
    const char* ga = gb + (size_t)pa * 16384u;
    const char* gbb = gb + (size_t)pb * 16384u;
    asm volatile(
        "s_nop 4\n\t"
        "s_mov_b32 m0, %0\n\ts_nop 0\n\tglobal_load_lds_dwordx4 %3, %1 nt\n\t"
        "s_add_u32 m0, m0, 1024\n\ts_nop 0\n\tglobal_load_lds_dwordx4 %4, %1 nt\n\t"
        "s_add_u32 m0, m0, 1024\n\ts_nop 0\n\tglobal_load_lds_dwordx4 %5, %1 nt\n\t"
        "s_add_u32 m0, m0, 1024\n\ts_nop 0\n\tglobal_load_lds_dwordx4 %6, %1 nt\n\t"
        "s_add_u32 m0, m0, 1024\n\ts_nop 0\n\tglobal_load_lds_dwordx4 %3, %2 nt\n\t"
        "s_add_u32 m0, m0, 1024\n\ts_nop 0\n\tglobal_load_lds_dwordx4 %4, %2 nt\n\t"
        "s_add_u32 m0, m0, 1024\n\ts_nop 0\n\tglobal_load_lds_dwordx4 %5, %2 nt\n\t"
        "s_add_u32 m0, m0, 1024\n\ts_nop 0\n\tglobal_load_lds_dwordx4 %6, %2 nt"
        :
        : "s"(dst), "s"(ga), "s"(gbb), "v"(voff[0]), "v"(voff[1]), "v"(voff[2]), "v"(voff[3])
        : "memory", "scc");
  };
  const unsigned wl_off = (unsigned)(uintptr_t)lds + (unsigned)wave * kWaveLds;

  f32x16 o[4][2];
#pragma unroll
  for (int d = 0; d < 4; ++d)
#pragma unroll
    for (int rb = 0; rb < 2; ++rb)
#pragma unroll
      for (int r = 0; r < 16; ++r) o[d][rb][r] = 0.f;
  float m[2] = {-INFINITY, -INFINITY}, l[2] = {0.f, 0.f};

  STAMP(2);
  if (n > 0) { issue(kbase, 0, wl_off); issue(vbase, 0, wl_off + 16384); }
  if (n > 1) { issue(kbase, 1, wl_off + 8192); issue(vbase, 1, wl_off + 24576); }

  unsigned kro[8];
#pragma unroll
  for (int s = 0; s < 8; ++s) kro[s] = off_b(r32, 2 * s + h);
  unsigned vro[4][2][2];
  {
    const int blk = (lane >> 4) & 1, qq = (lane & 15) >> 2, pp = lane & 3;
#pragma unroll
    for (int d = 0; d < 4; ++d)
#pragma unroll
      for (int ks = 0; ks < 2; ++ks)
#pragma unroll
        for (int t = 0; t < 2; ++t)
          vro[d][ks][t] = off_b(16 * ks + 8 * t + 4 * h + qq, 4 * d + 2 * blk + (pp >> 1)) + 8 * (pp & 1);
  }

  for (int it = 0; it < n; ++it) {
    char* kslot = wl + (it & 1) * 8192;
    char* vslot = wl + 16384 + (it & 1) * 8192;
#pragma unroll
    for (int rb = 0; rb < 2; ++rb) {
#pragma unroll
      for (int d = 0; d < 4; ++d) asm volatile("" : "+a"(o[d][rb]));
#pragma unroll
      for (int s = 0; s < 8; ++s) asm volatile("" : "+a"(qf[rb][s]));
    }
    if (it + 1 < n) WAIT_VM(24); else WAIT_VM(8);
    if (it < 8) STAMP(3 + it);
    bf16x8 kf[8];
#pragma unroll
    for (int s = 0; s < 8; ++s) kf[s] = *reinterpret_cast<const bf16x8*>(kslot + kro[s]);
    WAIT_LGKM0();
    if (it + 2 < n) issue(kbase, it + 2, wl_off + (it & 1) * 8192);
    f32x16 sc[2];
#pragma unroll
    for (int rb = 0; rb < 2; ++rb) {
#pragma unroll
      for (int r = 0; r < 16; ++r) sc[rb][r] = 0.f;
#pragma unroll
      for (int s = 0; s < 8; ++s) sc[rb] = __builtin_amdgcn_mfma_f32_32x32x16_bf16(kf[s], qf[rb][s], sc[rb], 0, 0, 0);
    }
    const int tok0 = p0 * kBS + it * kTile;
    if (tok0 + kTile > lim) {
#pragma unroll
      for (int rb = 0; rb < 2; ++rb)
#pragma unroll
        for (int r = 0; r < 16; ++r) {
          const int tok = tok0 + (r & 3) + 8 * (r >> 2) + 4 * h;
          sc[rb][r] = tok < lim ? sc[rb][r] : -INFINITY;
        }
    }
    bf16x8 pf[2][2];
    float mx[2];
#pragma unroll
    for (int rb = 0; rb < 2; ++rb) {
      float t = sc[rb][0];
#pragma unroll
      for (int r = 1; r < 16; ++r) t = fmaxf(t, sc[rb][r]);
      auto sw = __builtin_amdgcn_permlane32_swap(__float_as_uint(t), __float_as_uint(t), false, false);
      mx[rb] = fmaxf(__uint_as_float(sw[0]), __uint_as_float(sw[1])) * c2;
    }
#pragma unroll
    for (int rb = 0; rb < 2; ++rb)
#pragma unroll
      for (int d = 0; d < 4; ++d) asm volatile("" : "+a"(o[d][rb]));
    if (__any(mx[0] > m[0] + 8.f || mx[1] > m[1] + 8.f)) {
#pragma unroll
      for (int rb = 0; rb < 2; ++rb) {
        const float mn = fmaxf(m[rb], mx[rb]);
        const float alpha = __builtin_amdgcn_exp2f(m[rb] - mn);
        m[rb] = mn;
        l[rb] *= alpha;
        if (it > 0) {
#pragma unroll
          for (int d = 0; d < 4; ++d)
#pragma unroll
            for (int r = 0; r < 16; ++r) o[d][rb][r] *= alpha;
        }
      }
    }
#pragma unroll
    for (int rb = 0; rb < 2; ++rb)
#pragma unroll
      for (int d = 0; d < 4; ++d) asm volatile("" : "+a"(o[d][rb]));
#pragma unroll
    for (int rb = 0; rb < 2; ++rb) {
      float ps = 0.f;
#pragma unroll
      for (int r = 0; r < 16; ++r) {
        const float e = __builtin_amdgcn_exp2f(fmaf(sc[rb][r], c2, -m[rb]));
        sc[rb][r] = e;
        ps += e;
      }
      l[rb] += ps;
#pragma unroll
      for (int ks = 0; ks < 2; ++ks)
#pragma unroll
        for (int j = 0; j < 8; ++j) pf[rb][ks][j] = (__bf16)sc[rb][8 * ks + j];
    }
    if (it + 2 < n) WAIT_VM(24); else if (it + 1 < n) WAIT_VM(16); else WAIT_VM(0);
    bf16x8 vf[4][2];
#pragma unroll
    for (int d = 0; d < 4; ++d)
#pragma unroll
      for (int ks = 0; ks < 2; ++ks) {
        const bf16x4 lo = __builtin_amdgcn_ds_read_tr16_b64_v4bf16((lds_bf16x4_ptr)(vslot + vro[d][ks][0]));
        const bf16x4 hi = __builtin_amdgcn_ds_read_tr16_b64_v4bf16((lds_bf16x4_ptr)(vslot + vro[d][ks][1]));
        vf[d][ks] = __builtin_shufflevector(lo, hi, 0, 1, 2, 3, 4, 5, 6, 7);
      }
    WAIT_LGKM0();
    if (it + 2 < n) issue(vbase, it + 2, wl_off + 16384 + (it & 1) * 8192);
#pragma unroll
    for (int d = 0; d < 4; ++d)
#pragma unroll
      for (int ks = 0; ks < 2; ++ks)
#pragma unroll
        for (int rb = 0; rb < 2; ++rb)
          o[d][rb] = __builtin_amdgcn_mfma_f32_32x32x16_bf16(vf[d][ks], pf[rb][ks], o[d][rb], 0, 0, 0);
  }

  WAIT_VM(0);
  STAMP(12);
  float* oimg = reinterpret_cast<float*>(wl);
  float* mlimg = reinterpret_cast<float*>(wl + 64 * kOPitch * 4);
#pragma unroll
  for (int rb = 0; rb < 2; ++rb) {
    auto sw = __builtin_amdgcn_permlane32_swap(__float_as_uint(l[rb]), __float_as_uint(l[rb]), false, false);
    const float lt = __uint_as_float(sw[0]) + __uint_as_float(sw[1]);
    if (h == 0) *reinterpret_cast<f32x2*>(mlimg + (32 * rb + r32) * 2) = f32x2{m[rb], lt};
#pragma unroll
    for (int d = 0; d < 4; ++d)
#pragma unroll
      for (int g = 0; g < 4; ++g)
        *reinterpret_cast<f32x4*>(oimg + (32 * rb + r32) * kOPitch + 32 * d + 8 * g + 4 * h) =
            f32x4{o[d][rb][4 * g], o[d][rb][4 * g + 1], o[d][rb][4 * g + 2], o[d][rb][4 * g + 3]};
  }
  if (lane == 0) *reinterpret_cast<int*>(wl + 64 * kOPitch * 4 + 512) = active ? b : -1;
  __syncthreads();
  STAMP(13);
  {
    int bw[4];
#pragma unroll
    for (int w = 0; w < 4; ++w)
      bw[w] = __builtin_amdgcn_readfirstlane(*reinterpret_cast<const int*>(lds + w * kWaveLds + 64 * kOPitch * 4 + 512));
    const int d4 = (lane & 31) * 4;
    for (int lw = 0; lw < 4; ++lw) {
      if (bw[lw] < 0 || (lw > 0 && bw[lw - 1] == bw[lw])) continue;
      float* pod = po + (size_t)(blockIdx.x * 4 + lw) * kM * kD;
      float* pmd = pml + (size_t)(blockIdx.x * 4 + lw) * kM * 2;
#pragma unroll
      for (int ps = 0; ps < 8; ++ps) {
        const int row = 16 * wave + 2 * ps + (lane >> 5);
        float mw[4], lw_[4];
        float M = -INFINITY;
#pragma unroll
        for (int w = 0; w < 4; ++w) {
          const f32x2 v = *reinterpret_cast<const f32x2*>(lds + w * kWaveLds + 64 * kOPitch * 4 + row * 8);
          const bool in = bw[w] == bw[lw];
          mw[w] = in ? v[0] : -INFINITY;
          lw_[w] = in ? v[1] : 0.f;
          M = fmaxf(M, mw[w]);
        }
        f32x4 acc = {0.f, 0.f, 0.f, 0.f};
        float L = 0.f;
#pragma unroll
        for (int w = 0; w < 4; ++w) {
          if (bw[w] != bw[lw]) continue;
          const float sw = lw_[w] > 0.f ? __builtin_amdgcn_exp2f(mw[w] - M) : 0.f;
          const f32x4 v = *reinterpret_cast<const f32x4*>(lds + w * kWaveLds + (row * kOPitch + d4) * 4);
          acc += sw * v;
          L = fmaf(sw, lw_[w], L);
        }
        *reinterpret_cast<f32x4*>(pod + row * kD + d4) = acc;
        if ((lane & 31) == 0) *reinterpret_cast<f32x2*>(pmd + row * 2) = f32x2{M, L};
      }
    }
  }
  STAMP(14);
}

__global__ void __launch_bounds__(256)
attn_combine_kernel(const float* __restrict__ po, const float* __restrict__ pml, const int* __restrict__ cl,
                    __hip_bfloat16* __restrict__ out) {
  const int row = blockIdx.x * 8 + (threadIdx.x >> 5);
  const int d4 = (threadIdx.x & 31) * 4, lane = threadIdx.x & 63;
  const int b = (blockIdx.x * 8) / (kHQ * kSQ), rem = row % (kHQ * kSQ);
  const int kvh = rem / kM, R = rem % kM;
  Plan p;
  make_plan(cl, lane, p);
  const int li = p.row * 16 + b;
  const int g1 = __builtin_amdgcn_readlane(p.sc, li) - 1;
  const int g0 = g1 + 1 - __builtin_amdgcn_readlane(p.ns, li);
  const int w0 = g0 >> 2, ns = (g1 >> 2) - w0 + 1;
  float M = -INFINITY, L = 0.f;
  f32x4 acc = {0.f, 0.f, 0.f, 0.f};
  for (int j0 = 0; j0 < ns; j0 += 4) {
    f32x2 ml[4];
    f32x4 v[4];
#pragma unroll
    for (int k = 0; k < 4; ++k) {
      const int j = j0 + k < ns ? j0 + k : j0;
      const int lead = j == 0 ? (g0 & 3) : 0;
      const size_t idx = (size_t)(((w0 + j) * kHKV + kvh) * 4 + lead) * kM + R;
      ml[k] = *reinterpret_cast<const f32x2*>(pml + idx * 2);
      v[k] = *reinterpret_cast<const f32x4*>(po + idx * kD + d4);
    }
    float Mn = M;
#pragma unroll
    for (int k = 0; k < 4; ++k) {
      if (j0 + k >= ns) ml[k] = f32x2{-INFINITY, 0.f};
      Mn = fmaxf(Mn, ml[k][0]);
    }
    const float sc = __builtin_amdgcn_exp2f(M - Mn);
    acc *= sc;
    L *= sc;
#pragma unroll
    for (int k = 0; k < 4; ++k) {
      const float w = ml[k][1] > 0.f ? __builtin_amdgcn_exp2f(ml[k][0] - Mn) : 0.f;
      acc += w * v[k];
      L = fmaf(w, ml[k][1], L);
    }
    M = Mn;
  }
  const float inv = L > 0.f ? 1.f / L : 0.f;
  acc *= inv;
  bf16x4 r = {(__bf16)acc[0], (__bf16)acc[1], (__bf16)acc[2], (__bf16)acc[3]};
  *reinterpret_cast<bf16x4*>(reinterpret_cast<__bf16*>(out) + (size_t)row * kD + d4) = r;
}

}

extern "C" void kernel_launch(void* const* d_in, const int* in_sizes, int n_in, void* d_out, int out_size,
                              void* d_ws, size_t ws_size, hipStream_t stream) {
  const __hip_bfloat16* q = (const __hip_bfloat16*)d_in[0];
  const __hip_bfloat16* kc = (const __hip_bfloat16*)d_in[1];
  const __hip_bfloat16* vc = (const __hip_bfloat16*)d_in[2];
  const int* bt = (const int*)d_in[3];
  const int* cl = (const int*)d_in[4];
  float* po = (float*)d_ws;
  float* pml = po + (size_t)kNWG * 4 * kM * kD;
  (void)hipFuncSetAttribute((const void*)attn_split_kernel, hipFuncAttributeMaxDynamicSharedMemorySize, kLdsBytes);
  hipLaunchKernelGGL(attn_split_kernel, dim3(kNWG), dim3(256), kLdsBytes, stream, q, kc, vc, bt, cl, po, pml);
  hipLaunchKernelGGL(attn_combine_kernel, dim3(kB * kHQ * kSQ / 8), dim3(256), 0, stream, po, pml, cl,
                     (__hip_bfloat16*)d_out);
}
```

```cpp
#include <hip/hip_runtime.h>
#include <hip/hip_bf16.h>
#include <stdint.h>

namespace {

constexpr int kB = 16, kHQ = 32, kHKV = 4, kSQ = 8, kD = 128, kBS = 16, kNBPS = 256;
constexpr int kG = kHQ / kHKV;
constexpr int kM = kG * kSQ;
constexpr int kSKV = kNBPS * kBS;
constexpr int kTile = 32;
constexpr int kNWG = 256;
constexpr int kSlots = kNWG / kHKV;
constexpr int kWaveLds = 40960;
constexpr int kLdsBytes = 4 * kWaveLds;
constexpr int kOPitch = 132;

using bf16x8 = __attribute__((ext_vector_type(8))) __bf16;
using bf16x4 = __attribute__((ext_vector_type(4))) __bf16;
using bf16x2 = __attribute__((ext_vector_type(2))) __bf16;
using f32x16 = __attribute__((ext_vector_type(16))) float;
using f32x4 = __attribute__((ext_vector_type(4))) float;
using f32x2 = __attribute__((ext_vector_type(2))) float;
using u32x4 = __attribute__((ext_vector_type(4))) unsigned;
typedef __attribute__((address_space(3))) bf16x4* lds_bf16x4_ptr;

__device__ __forceinline__ unsigned off_b(unsigned row, unsigned ch) {
  return 256u * row + 16u * (ch ^ (((row & 3u) << 2) | ((row >> 2) & 3u)));
}

__device__ __forceinline__ int eff_len(int c) { return c <= 0 ? kSKV : (c > kSKV ? kSKV : c); }
__device__ __forceinline__ int row_scan(int v) {
  v += __builtin_amdgcn_update_dpp(0, v, 0x111, 0xf, 0xf, true);
  v += __builtin_amdgcn_update_dpp(0, v, 0x112, 0xf, 0xf, true);
  v += __builtin_amdgcn_update_dpp(0, v, 0x114, 0xf, 0xf, true);
  v += __builtin_amdgcn_update_dpp(0, v, 0x118, 0xf, 0xf, true);
  return v;
}
struct Plan {
  int nt, ns, sc, c;
  int row;
};
__device__ __forceinline__ void make_plan(const int* __restrict__ cl, int lane, Plan& p) {
  p.c = cl[lane & 15];
  p.nt = (eff_len(p.c) + kTile - 1) >> 5;
  const int total = __builtin_amdgcn_readlane(row_scan(p.nt), 15);
  int C0 = (total + kSlots - 1) / kSlots;
  for (;;) {
    const unsigned cand = (unsigned)(C0 + (lane >> 4));
    p.ns = (int)(((unsigned)p.nt + cand - 1u) / cand);
    p.sc = row_scan(p.ns);
    const unsigned long long ok = __ballot((lane & 15) == 15 && p.sc <= kSlots);
    if (ok) { p.row = (__ffsll((long long)ok) - 1) >> 4; break; }
    C0 += 4;
  }
}

#define WAIT_VM(n) asm volatile("s_waitcnt vmcnt(" #n ")" ::: "memory")
#define WAIT_LGKM0() asm volatile("s_waitcnt lgkmcnt(0)" ::: "memory")

__global__ void __launch_bounds__(256, 1)
attn_split_kernel(const __hip_bfloat16* __restrict__ q, const __hip_bfloat16* __restrict__ kc,
                  const __hip_bfloat16* __restrict__ vc, const int* __restrict__ bt,
                  const int* __restrict__ cl, float* __restrict__ po, float* __restrict__ pml) {
  extern __shared__ __attribute__((aligned(16))) char lds[];
  const int tid = threadIdx.x, lane = tid & 63;
  const int wave = __builtin_amdgcn_readfirstlane(tid >> 6);
  const int r32 = lane & 31, h = lane >> 5;
  const int kvh = blockIdx.x & (kHKV - 1), slot = blockIdx.x / kHKV;

  {
    const char* src = reinterpret_cast<const char*>(bt) + wave * 4096;
    const unsigned dst = (unsigned)(uintptr_t)lds + (unsigned)wave * kWaveLds + 32768u;
    asm volatile(
        "s_mov_b32 m0, %0\n\ts_nop 0\n\tglobal_load_lds_dwordx4 %2, %1\n\t"
        "global_load_lds_dwordx4 %2, %1 offset:1024\n\t"
        "global_load_lds_dwordx4 %2, %1 offset:2048\n\t"
        "global_load_lds_dwordx4 %2, %1 offset:3072"
        :
        : "s"(dst), "s"(src), "v"((unsigned)lane * 16u)
        : "memory", "scc");
  }
  Plan p;
  make_plan(cl, lane, p);
  int b, tile0, tile1, clb;
  bool active;
  {
    const unsigned long long hit = __ballot((lane >> 4) == p.row && slot >= p.sc - p.ns && slot < p.sc);
    active = hit != 0;
    const int li = active ? __ffsll((long long)hit) - 1 : 0;
    b = li & 15;
    const int nt = __builtin_amdgcn_readlane(p.nt, li), ns = __builtin_amdgcn_readlane(p.ns, li);
    const int j = slot - (__builtin_amdgcn_readlane(p.sc, li) - ns);
    clb = __builtin_amdgcn_readlane(p.c, li);
    const int base = nt / ns, rem = nt - base * ns;
    tile0 = j * base + (j < rem ? j : rem);
    tile1 = tile0 + base + (j < rem ? 1 : 0);
  }
  WAIT_VM(0);
  __syncthreads();
  if (!active) return;
  const int len = eff_len(clb);
  const float c2 = clb > 0 ? 0.08838834764831845f * 1.4426950408889634f : 0.f;

  const int n = (tile1 - tile0 - wave + 3) >> 2;
  int btv = 0;
  {
    const int pg = 2 * (tile0 + wave + 4 * (lane >> 1)) + (lane & 1);
    if (pg < kNBPS) btv = *reinterpret_cast<const int*>(lds + (b >> 2) * kWaveLds + 32768 + (b & 3) * 1024 + pg * 4);
  }
  bf16x8 qf[2][8];
  {
    const char* qb = reinterpret_cast<const char*>(q + (size_t)(b * kHQ + kvh * kG) * kSQ * kD);
    const unsigned qo = (unsigned)r32 * 256u + 16u * (unsigned)h;
#pragma unroll
    for (int rb = 0; rb < 2; ++rb)
#pragma unroll
      for (int s = 0; s < 8; ++s)
        asm volatile("global_load_dwordx4 %0, %1, %2 offset:%3" : "=a"(qf[rb][s]) : "v"(qo), "s"(qb + rb * 8192), "i"(32 * s));
  }

  unsigned voff[4];
#pragma unroll
  for (int i = 0; i < 4; ++i)
    voff[i] = (unsigned)(4 * i + (lane >> 4)) * 1024u + ((unsigned)((lane & 15) ^ (((lane >> 4) << 2) | i)) << 4);

  char* const wl = lds + wave * kWaveLds;
  const char* const kbase = reinterpret_cast<const char*>(kc) + kvh * 256;
  const char* const vbase = reinterpret_cast<const char*>(vc) + kvh * 256;

  auto issue = [&](const char* gb, int it, unsigned dst) {
    const unsigned pa = (unsigned)__builtin_amdgcn_readlane(btv, 2 * it);
    const unsigned pb = (unsigned)__builtin_amdgcn_readlane(btv, 2 * it + 1);
    const char* ga = gb + (size_t)pa * 16384u;
    const char* gbb = gb + (size_t)pb * 16384u;
    asm volatile(
        "s_nop 4\n\t"
        "s_mov_b32 m0, %0\n\ts_nop 0\n\tglobal_load_lds_dwordx4 %3, %1 nt\n\t"
        "s_add_u32 m0, m0, 1024\n\ts_nop 0\n\tglobal_load_lds_dwordx4 %4, %1 nt\n\t"
        "s_add_u32 m0, m0, 1024\n\ts_nop 0\n\tglobal_load_lds_dwordx4 %5, %1 nt\n\t"
        "s_add_u32 m0, m0, 1024\n\ts_nop 0\n\tglobal_load_lds_dwordx4 %6, %1 nt\n\t"
        "s_add_u32 m0, m0, 1024\n\ts_nop 0\n\tglobal_load_lds_dwordx4 %3, %2 nt\n\t"
        "s_add_u32 m0, m0, 1024\n\ts_nop 0\n\tglobal_load_lds_dwordx4 %4, %2 nt\n\t"
        "s_add_u32 m0, m0, 1024\n\ts_nop 0\n\tglobal_load_lds_dwordx4 %5, %2 nt\n\t"
        "s_add_u32 m0, m0, 1024\n\ts_nop 0\n\tglobal_load_lds_dwordx4 %6, %2 nt"
        :
        : "s"(dst), "s"(ga), "s"(gbb), "v"(voff[0]), "v"(voff[1]), "v"(voff[2]), "v"(voff[3])
        : "memory", "scc");
  };
  const unsigned wl_off = (unsigned)(uintptr_t)lds + (unsigned)wave * kWaveLds;

  f32x16 o[4][2];
#pragma unroll
  for (int d = 0; d < 4; ++d)
#pragma unroll
    for (int rb = 0; rb < 2; ++rb)
#pragma unroll
      for (int r = 0; r < 16; ++r) o[d][rb][r] = 0.f;
  float m[2] = {-INFINITY, -INFINITY}, l[2] = {0.f, 0.f};

  if (n > 0) { issue(kbase, 0, wl_off); issue(vbase, 0, wl_off + 16384); }
  if (n > 1) { issue(kbase, 1, wl_off + 8192); issue(vbase, 1, wl_off + 24576); }

  unsigned kro[8];
#pragma unroll
  for (int s = 0; s < 8; ++s) kro[s] = off_b(r32, 2 * s + h);
  unsigned vro[4][2][2];
  {
    const int blk = (lane >> 4) & 1, qq = (lane & 15) >> 2, pp = lane & 3;
#pragma unroll
    for (int d = 0; d < 4; ++d)
#pragma unroll
      for (int ks = 0; ks < 2; ++ks)
#pragma unroll
        for (int t = 0; t < 2; ++t)
          vro[d][ks][t] = off_b(16 * ks + 8 * t + 4 * h + qq, 4 * d + 2 * blk + (pp >> 1)) + 8 * (pp & 1);
  }

  for (int it = 0; it < n; ++it) {
    char* kslot = wl + (it & 1) * 8192;
    char* vslot = wl + 16384 + (it & 1) * 8192;
#pragma unroll
    for (int rb = 0; rb < 2; ++rb) {
#pragma unroll
      for (int d = 0; d < 4; ++d) asm volatile("" : "+a"(o[d][rb]));
#pragma unroll
      for (int s = 0; s < 8; ++s) asm volatile("" : "+a"(qf[rb][s]));
    }
    if (it + 1 < n) WAIT_VM(24); else WAIT_VM(8);
    bf16x8 kf[8];
#pragma unroll
    for (int s = 0; s < 8; ++s) kf[s] = *reinterpret_cast<const bf16x8*>(kslot + kro[s]);
    WAIT_LGKM0();
    if (it + 2 < n) issue(kbase, it + 2, wl_off + (it & 1) * 8192);
    f32x16 sc[2];
#pragma unroll
    for (int rb = 0; rb < 2; ++rb) {
#pragma unroll
      for (int r = 0; r < 16; ++r) sc[rb][r] = 0.f;
#pragma unroll
      for (int s = 0; s < 8; ++s) sc[rb] = __builtin_amdgcn_mfma_f32_32x32x16_bf16(kf[s], qf[rb][s], sc[rb], 0, 0, 0);
    }
    const int tok0 = (tile0 + wave + 4 * it) * kTile;
    if (tok0 + kTile > len) {
#pragma unroll
      for (int rb = 0; rb < 2; ++rb)
#pragma unroll
        for (int r = 0; r < 16; ++r) {
          const int tok = tok0 + (r & 3) + 8 * (r >> 2) + 4 * h;
          sc[rb][r] = tok < len ? sc[rb][r] : -INFINITY;
        }
    }
    bf16x8 pf[2][2];
    float mx[2];
#pragma unroll
    for (int rb = 0; rb < 2; ++rb) {
      float t = sc[rb][0];
#pragma unroll
      for (int r = 1; r < 16; ++r) t = fmaxf(t, sc[rb][r]);
      auto sw = __builtin_amdgcn_permlane32_swap(__float_as_uint(t), __float_as_uint(t), false, false);
      mx[rb] = fmaxf(__uint_as_float(sw[0]), __uint_as_float(sw[1])) * c2;
    }
#pragma unroll
    for (int rb = 0; rb < 2; ++rb)
#pragma unroll
      for (int d = 0; d < 4; ++d) asm volatile("" : "+a"(o[d][rb]));
    if (__any(mx[0] > m[0] + 8.f || mx[1] > m[1] + 8.f)) {
#pragma unroll
      for (int rb = 0; rb < 2; ++rb) {
        const float mn = fmaxf(m[rb], mx[rb]);
        const float alpha = __builtin_amdgcn_exp2f(m[rb] - mn);
        m[rb] = mn;
        l[rb] *= alpha;
        if (it > 0) {
#pragma unroll
          for (int d = 0; d < 4; ++d)
#pragma unroll
            for (int r = 0; r < 16; ++r) o[d][rb][r] *= alpha;
        }
      }
    }
#pragma unroll
    for (int rb = 0; rb < 2; ++rb)
#pragma unroll
      for (int d = 0; d < 4; ++d) asm volatile("" : "+a"(o[d][rb]));
#pragma unroll
    for (int rb = 0; rb < 2; ++rb) {
      float ps = 0.f;
#pragma unroll
      for (int r = 0; r < 16; ++r) {
        const float e = __builtin_amdgcn_exp2f(fmaf(sc[rb][r], c2, -m[rb]));
        sc[rb][r] = e;
        ps += e;
      }
      l[rb] += ps;
#pragma unroll
      for (int ks = 0; ks < 2; ++ks)
#pragma unroll
        for (int j = 0; j < 8; ++j) pf[rb][ks][j] = (__bf16)sc[rb][8 * ks + j];
    }
    if (it + 2 < n) WAIT_VM(24); else if (it + 1 < n) WAIT_VM(16); else WAIT_VM(0);
    bf16x8 vf[4][2];
#pragma unroll
    for (int d = 0; d < 4; ++d)
#pragma unroll
      for (int ks = 0; ks < 2; ++ks) {
        const bf16x4 lo = __builtin_amdgcn_ds_read_tr16_b64_v4bf16((lds_bf16x4_ptr)(vslot + vro[d][ks][0]));
        const bf16x4 hi = __builtin_amdgcn_ds_read_tr16_b64_v4bf16((lds_bf16x4_ptr)(vslot + vro[d][ks][1]));
        vf[d][ks] = __builtin_shufflevector(lo, hi, 0, 1, 2, 3, 4, 5, 6, 7);
      }
    WAIT_LGKM0();
    if (it + 2 < n) issue(vbase, it + 2, wl_off + 16384 + (it & 1) * 8192);
#pragma unroll
    for (int d = 0; d < 4; ++d)
#pragma unroll
      for (int ks = 0; ks < 2; ++ks)
#pragma unroll
        for (int rb = 0; rb < 2; ++rb)
          o[d][rb] = __builtin_amdgcn_mfma_f32_32x32x16_bf16(vf[d][ks], pf[rb][ks], o[d][rb], 0, 0, 0);
  }

  WAIT_VM(0);
  float* oimg = reinterpret_cast<float*>(wl);
  float* mlimg = reinterpret_cast<float*>(wl + 64 * kOPitch * 4);
#pragma unroll
  for (int rb = 0; rb < 2; ++rb) {
    auto sw = __builtin_amdgcn_permlane32_swap(__float_as_uint(l[rb]), __float_as_uint(l[rb]), false, false);
    const float lt = __uint_as_float(sw[0]) + __uint_as_float(sw[1]);
    if (h == 0) *reinterpret_cast<f32x2*>(mlimg + (32 * rb + r32) * 2) = f32x2{m[rb], lt};
#pragma unroll
    for (int d = 0; d < 4; ++d)
#pragma unroll
      for (int g = 0; g < 4; ++g)
        *reinterpret_cast<f32x4*>(oimg + (32 * rb + r32) * kOPitch + 32 * d + 8 * g + 4 * h) =
            f32x4{o[d][rb][4 * g], o[d][rb][4 * g + 1], o[d][rb][4 * g + 2], o[d][rb][4 * g + 3]};
  }
  __syncthreads();
  {
    const int d4 = (lane & 31) * 4;
#pragma unroll
    for (int ps = 0; ps < 8; ++ps) {
      const int row = 16 * wave + 2 * ps + (lane >> 5);
      float mw[4], lw[4];
      float M = -INFINITY;
#pragma unroll
      for (int w = 0; w < 4; ++w) {
        const f32x2 v = *reinterpret_cast<const f32x2*>(lds + w * kWaveLds + 64 * kOPitch * 4 + row * 8);
        mw[w] = v[0]; lw[w] = v[1];
        M = fmaxf(M, mw[w]);
      }
      f32x4 acc = {0.f, 0.f, 0.f, 0.f};
      float L = 0.f;
#pragma unroll
      for (int w = 0; w < 4; ++w) {
        const float sw = lw[w] > 0.f ? __builtin_amdgcn_exp2f(mw[w] - M) : 0.f;
        const f32x4 v = *reinterpret_cast<const f32x4*>(lds + w * kWaveLds + (row * kOPitch + d4) * 4);
        acc += sw * v;
        L = fmaf(sw, lw[w], L);
      }
      *reinterpret_cast<f32x4*>(po + ((size_t)blockIdx.x * kM + row) * kD + d4) = acc;
      if ((lane & 31) == 0) *reinterpret_cast<f32x2*>(pml + ((size_t)blockIdx.x * kM + row) * 2) = f32x2{M, L};
    }
  }
}

__global__ void __launch_bounds__(256)
attn_combine_kernel(const float* __restrict__ po, const float* __restrict__ pml, const int* __restrict__ cl,
                    __hip_bfloat16* __restrict__ out) {
  const int row = blockIdx.x * 8 + (threadIdx.x >> 5);
  const int d4 = (threadIdx.x & 31) * 4, lane = threadIdx.x & 63;
  const int b = (blockIdx.x * 8) / (kHQ * kSQ), rem = row % (kHQ * kSQ);
  const int kvh = rem / kM, R = rem % kM;
  Plan p;
  make_plan(cl, lane, p);
  const int li = p.row * 16 + b;
  const int ns = __builtin_amdgcn_readlane(p.ns, li);
  const int first = __builtin_amdgcn_readlane(p.sc, li) - ns;
  float M = -INFINITY, L = 0.f;
  f32x4 acc = {0.f, 0.f, 0.f, 0.f};
  for (int j0 = 0; j0 < ns; j0 += 4) {
    f32x2 ml[4];
    f32x4 v[4];
#pragma unroll
    for (int k = 0; k < 4; ++k) {
      const int j = j0 + k < ns ? j0 + k : j0;
      const size_t idx = (size_t)((first + j) * kHKV + kvh) * kM + R;
      ml[k] = *reinterpret_cast<const f32x2*>(pml + idx * 2);
      v[k] = *reinterpret_cast<const f32x4*>(po + idx * kD + d4);
    }
    float Mn = M;
#pragma unroll
    for (int k = 0; k < 4; ++k) {
      if (j0 + k >= ns) ml[k] = f32x2{-INFINITY, 0.f};
      Mn = fmaxf(Mn, ml[k][0]);
    }
    const float sc = __builtin_amdgcn_exp2f(M - Mn);
    acc *= sc;
    L *= sc;
#pragma unroll
    for (int k = 0; k < 4; ++k) {
      const float w = ml[k][1] > 0.f ? __builtin_amdgcn_exp2f(ml[k][0] - Mn) : 0.f;
      acc += w * v[k];
      L = fmaf(w, ml[k][1], L);
    }
    M = Mn;
  }
  const float inv = L > 0.f ? 1.f / L : 0.f;
  acc *= inv;
  bf16x4 r = {(__bf16)acc[0], (__bf16)acc[1], (__bf16)acc[2], (__bf16)acc[3]};
  *reinterpret_cast<bf16x4*>(reinterpret_cast<__bf16*>(out) + (size_t)row * kD + d4) = r;
}

}

extern "C" void kernel_launch(void* const* d_in, const int* in_sizes, int n_in, void* d_out, int out_size,
                              void* d_ws, size_t ws_size, hipStream_t stream) {
  const __hip_bfloat16* q = (const __hip_bfloat16*)d_in[0];
  const __hip_bfloat16* kc = (const __hip_bfloat16*)d_in[1];
  const __hip_bfloat16* vc = (const __hip_bfloat16*)d_in[2];
  const int* bt = (const int*)d_in[3];
  const int* cl = (const int*)d_in[4];
  float* po = (float*)d_ws;
  float* pml = po + (size_t)kNWG * kM * kD;
  (void)hipFuncSetAttribute((const void*)attn_split_kernel, hipFuncAttributeMaxDynamicSharedMemorySize, kLdsBytes);
  hipLaunchKernelGGL(attn_split_kernel, dim3(kNWG), dim3(256), kLdsBytes, stream, q, kc, vc, bt, cl, po, pml);
  hipLaunchKernelGGL(attn_combine_kernel, dim3(kB * kHQ * kSQ / 8), dim3(256), 0, stream, po, pml, cl,
                     (__hip_bfloat16*)d_out);
}
```
